```python
import torch

def _softmax(x, axis=-1):
    return torch.softmax(x, dim=axis)

def _gelu(x):
    return torch.nn.functional.gelu(x, approximate='tanh')

def _silu(x):
    return torch.nn.functional.silu(x)

def _sigmoid(x):
    return torch.sigmoid(x)

def _layernorm(x, axis=-1, eps=1e-05):
    m = x.mean(dim=axis, keepdim=True)
    v = x.var(dim=axis, keepdim=True, unbiased=False)
    return (x - m) / torch.sqrt(v + eps)

def _scatter_add(tgt, idx, src, axis=0):
    return tgt.clone().index_add_(axis, idx, src)

def _segment_sum(src, seg, n):
    out = torch.zeros((n,) + src.shape[1:], dtype=src.dtype)
    return out.index_add_(0, seg, src)

def _pad(x, pw):
    flat = tuple((v for pair in reversed(pw) for v in pair))
    return torch.nn.functional.pad(x, flat)
kernel_name = 'gated_mix__softmax_full_mha__mixer_gmlp__prompt'

def setup_inputs():

    _w = lambda a: (lambda t: t.to(torch.bfloat16) if t.is_floating_point() else t)(torch.as_tensor(a))

    def _A_setup():
        B, S, D = (1, 2048, 640)
        return {'x': _w((lambda *s: torch.randn(*s, dtype=torch.bfloat16) * 0.05)(B, S, D)), 'wqkv': _w((lambda *s: torch.randn(*s, dtype=torch.bfloat16) * 0.8)(D, 3 * D)), 'wo': _w((lambda *s: torch.randn(*s, dtype=torch.bfloat16) * 0.1)(D, D))}
    _A_in = _A_setup()

    def _B_setup():
        B, S, D, F = (1, 2048, 640, 1728)
        Fh = F // 2
        return {'x': _w((lambda *s: torch.randn(*s, dtype=torch.bfloat16) * 0.05)(B, S, D)), 'w_in': _w((lambda *s: torch.randn(*s, dtype=torch.bfloat16) * 0.4)(D, 2 * Fh)), 'w_sp': _w(0.01 * (lambda *s: torch.randn(*s, dtype=torch.bfloat16) * 0.4)(S, S)), 'b_sp': _w(1.0 + 0.01 * (lambda *s: torch.randn(*s, dtype=torch.bfloat16) * 0.05)(S)), 'w_out': _w((lambda *s: torch.randn(*s, dtype=torch.bfloat16) * 0.05)(Fh, D))}
    _B_in = _B_setup()
    return {'A_x': _A_in['x'], 'A_wqkv': _A_in['wqkv'], 'A_wo': _A_in['wo'], 'B_x': _B_in['x'], 'B_w_in': _B_in['w_in'], 'B_w_sp': _B_in['w_sp'], 'B_b_sp': _B_in['b_sp'], 'B_w_out': _B_in['w_out'], 'gate_w': _w((lambda *s: torch.randn(*s, dtype=torch.bfloat16) * 0.05)(640) * 16.0)}

def reference(A_x, A_wqkv, A_wo, B_x, B_w_in, B_w_sp, B_b_sp, B_w_out, gate_w):

    def _A_ref(x, wqkv, wo):
        B, S, D = x.shape
        H = 32
        Dh = D // H
        qkv = (lambda x, *d: x.permute(*d))((x @ wqkv).reshape(B, S, 3, H, Dh), 2, 0, 3, 1, 4)
        q, k, v = (qkv[0], qkv[1], qkv[2])
        scale = Dh ** (-0.5)
        scores = torch.einsum('bhqd,bhkd->bhqk', q, k) * scale
        p = _softmax(scores, axis=-1)
        o = torch.einsum('bhqk,bhkd->bhqd', p, v)
        return (lambda x, *d: x.permute(*d))(o, 0, 2, 1, 3).reshape(B, S, D) @ wo

    def _B_ref(x, w_in, w_sp, b_sp, w_out):
        B, S, D = x.shape
        F = w_in.shape[-1]
        Fh = F // 2
        z = _gelu(torch.einsum('bsd,df->bsf', x, w_in))
        z1 = z[..., :Fh].clone()
        z2 = _layernorm(z[..., Fh:], axis=-1)
        g = torch.einsum('ts,bsf->btf', w_sp, z2) + b_sp[None, :, None]
        return torch.einsum('bsf,fd->bsd', z1 * g, w_out)
    x = A_x
    g = _sigmoid((x * gate_w).sum(-1, keepdims=True))
    a = _A_ref(x, A_wqkv, A_wo)
    b = _B_ref(x, B_w_in, B_w_sp, B_b_sp, B_w_out)
    return g * a + (1 - g) * b
```

```cpp
#include <hip/hip_runtime.h>
#include <hip/hip_bf16.h>

typedef float    f32x2  __attribute__((ext_vector_type(2)));
typedef float    f32x4  __attribute__((ext_vector_type(4)));
typedef float    f32x16 __attribute__((ext_vector_type(16)));
typedef __bf16   bf16x2 __attribute__((ext_vector_type(2)));
typedef __bf16   bf16x8 __attribute__((ext_vector_type(8)));
typedef __bf16   bf16x16 __attribute__((ext_vector_type(16)));
typedef short    s16x4  __attribute__((ext_vector_type(4)));
typedef short    s16x8  __attribute__((ext_vector_type(8)));
typedef unsigned u32x2  __attribute__((ext_vector_type(2)));
typedef unsigned u32x4  __attribute__((ext_vector_type(4)));
typedef unsigned u32x8  __attribute__((ext_vector_type(8)));
typedef int      i32x4  __attribute__((ext_vector_type(4)));
typedef unsigned long long u64;
typedef __amdgpu_buffer_rsrc_t rsrc_t;
#define LAS __attribute__((address_space(3)))
#define GAS __attribute__((address_space(1)))
typedef LAS char* lds_ptr;
#define DI __device__ __forceinline__
#define SBAR() __builtin_amdgcn_sched_barrier(0)
#define RLX_AGENT __ATOMIC_RELAXED, __HIP_MEMORY_SCOPE_AGENT
#define AUX_SC1 16

constexpr unsigned WS_EP = 0;
constexpr unsigned WS_GATE = 4096;
constexpr unsigned WS_FQ = 16384;
constexpr unsigned WS_FZT = WS_FQ + 128 * 128;
constexpr unsigned WS_FZ1 = WS_FZT + 128 * 128;
constexpr unsigned WS_FW = WS_FZ1 + 512 * 128;
constexpr unsigned WS_FU = WS_FW + 384 * 128;
constexpr unsigned WS_FO = WS_FU + 256 * 128;
constexpr unsigned WS_ST = WS_FO + 1024 * 128;
constexpr unsigned WS_Q = WS_ST + 262144;
constexpr unsigned WS_K = WS_Q + 16 * 64 * 2560;
constexpr unsigned WS_V = WS_K + 16 * 64 * 2560;
constexpr unsigned WS_Z1 = WS_V + 32 * 64 * 1280;
constexpr unsigned WS_ZT = WS_Z1 + 32 * 27 * 4096;
constexpr unsigned WS_WF = WS_ZT + 64 * 54 * 1024;
constexpr unsigned WS_U = WS_WF + 47 * 40 * 1024;
constexpr unsigned WS_O = WS_U + 32 * 27 * 4096;
constexpr unsigned WS_END = WS_O + 32 * 20 * 4096;
constexpr unsigned SPIN_CAP = 1u << 18;
constexpr unsigned K1_SLOT = 49152, L_GW = 3 * K1_SLOT, L_WR = 0, L_BX = 0, L_AX = 98304, L_AR = 98304, L_CNT = 163776, LDS_BYTES = 163840;

DI int rfl(int x) { return __builtin_amdgcn_readfirstlane(x); }
DI rsrc_t mk_rsrc(const void* p, unsigned bytes) { return __builtin_amdgcn_make_buffer_rsrc((void*)p, 0, bytes, 0x00020000); }
DI i32x4 mk_srd(const void* p, unsigned bytes) { const u64 a = (u64)p; i32x4 r; r[0] = (int)a; r[1] = (int)(a >> 32) & 0xffff; r[2] = (int)bytes; r[3] = 0x00020000; return r; }
DI unsigned pk2(float lo, float hi) { f32x2 v = {lo, hi}; return __builtin_bit_cast(unsigned, __builtin_convertvector(v, bf16x2)); }
DI u32x2 pk4(f32x4 v) { u32x2 r = {pk2(v[0], v[1]), pk2(v[2], v[3])}; return r; }
DI u32x4 pk8(f32x4 a, f32x4 b) { u32x4 r = {pk2(a[0], a[1]), pk2(a[2], a[3]), pk2(b[0], b[1]), pk2(b[2], b[3])}; return r; }
DI float bflo(unsigned w) { return __uint_as_float(w << 16); }
DI float bfhi(unsigned w) { return __uint_as_float(w & 0xffff0000u); }
DI f32x4 mfma16(bf16x8 a, bf16x8 b, f32x4 c) { return __builtin_amdgcn_mfma_f32_16x16x32_bf16(a, b, c, 0, 0, 0); }
template <int C> DI float dppf(float v) { return __int_as_float(__builtin_amdgcn_update_dpp(0, __float_as_int(v), C, 0xf, 0xf, true)); }
DI f32x2 gelu2(f32x2 x) {
  const f32x2 c2 = {-0.10294324f, -0.10294324f}, c1 = {-2.30220820f, -2.30220820f};
  const f32x2 a = x * __builtin_elementwise_fma(x * x, c2, c1);
  f32x2 d = {__builtin_amdgcn_exp2f(a[0]), __builtin_amdgcn_exp2f(a[1])}; d += 1.0f;
  const f32x2 r = {__builtin_amdgcn_rcpf(d[0]), __builtin_amdgcn_rcpf(d[1])};
  return x * r; }
DI f32x4 gelu4(f32x4 x) { const f32x2 a = gelu2(x.xy), b = gelu2(x.zw); f32x4 r = {a[0], a[1], b[0], b[1]}; return r; }
DI u64 next_tag(u64 e) { const u64 t = e + 1; return t ? t : 1; }
DI void dma16(unsigned lds, unsigned voff, i32x4 srd, unsigned soff) {
  unsigned keep;
  asm volatile("s_mov_b32 %0, m0\n\ts_mov_b32 m0, %1\n\ts_nop 0\n\tbuffer_load_dwordx4 %2, %3, %4 offen lds\n\ts_mov_b32 m0, %0"
               : "=&s"(keep) : "s"(lds), "v"(voff), "s"(srd), "s"(soff) : "memory"); }
DI void publish(char* ws, unsigned off, u64 tag, int lane) { asm volatile("s_waitcnt vmcnt(0)" ::: "memory"); if (lane == 0) __hip_atomic_store((GAS u64*)(ws + off), tag, RLX_AGENT); }
template <int VM = 0> DI void publish_n(char* ws, unsigned off, u64 tag, int lane, unsigned cnt, unsigned n) {
  if (lane == 0) { unsigned v; asm volatile("s_waitcnt vmcnt(%3)\n\tds_add_rtn_u32 %0, %1, %2\n\ts_waitcnt lgkmcnt(0)" : "=&v"(v) : "v"(cnt), "v"(1u), "i"(VM) : "memory");
    if (v == n - 1) __hip_atomic_store((GAS u64*)(ws + off), tag, RLX_AGENT); } }
DI void poll64(i32x4 srd, unsigned off, u64 tag) {
#pragma nounroll
  for (unsigned s = 0; s < SPIN_CAP; ++s) { u32x2 v; asm volatile("buffer_load_dwordx2 %0, %1, %2, 0 offen sc1\n\ts_waitcnt vmcnt(0)" : "=&v"(v) : "v"(off), "s"(srd) : "memory");
    if (v[0] == (unsigned)tag && v[1] == (unsigned)(tag >> 32)) off = 0x7fff0000u;
    if (__all(off == 0x7fff0000u)) break; __builtin_amdgcn_s_sleep(1); }
  __builtin_amdgcn_fence(__ATOMIC_ACQUIRE, "wavefront"); }
DI void st16(u32x4 v, rsrc_t r, unsigned off) { __builtin_amdgcn_raw_buffer_store_b128(v, r, off, 0, AUX_SC1); }
DI void st8(u32x2 v, rsrc_t r, unsigned off) { __builtin_amdgcn_raw_buffer_store_b64(v, r, off, 0, AUX_SC1); }
DI bf16x8 ldfrag(rsrc_t r, unsigned voff, unsigned soff) { return __builtin_bit_cast(bf16x8, __builtin_amdgcn_raw_buffer_load_b128(r, voff, soff, AUX_SC1)); }
DI bf16x8 ldfrag2(rsrc_t r, unsigned voff, unsigned d, unsigned soff) {
  const u32x2 a = __builtin_amdgcn_raw_buffer_load_b64(r, voff, soff, AUX_SC1), b = __builtin_amdgcn_raw_buffer_load_b64(r, voff + d, soff, AUX_SC1);
  return __builtin_bit_cast(bf16x8, u32x4{a[0], a[1], b[0], b[1]}); }
DI void lds_arrive(unsigned a, int lane) { if (lane == 0) asm volatile("s_waitcnt lgkmcnt(0)\n\tds_add_u32 %0, %1" :: "v"(a), "v"(1u) : "memory"); }
template <int SL = 0> DI void lds_wait4(unsigned a, unsigned n = 4) {
  unsigned v, t, c = SPIN_CAP * 8;
  asm volatile(".LLW_%=:\n\tds_read_b32 %0, %3\n\ts_waitcnt lgkmcnt(0)\n\tv_readfirstlane_b32 %1, %0\n\ts_sub_u32 %2, %2, 1\n\ts_cselect_b32 %1, -1, %1\n\t"
               "s_sleep %5\n\ts_cmp_lt_u32 %1, %4\n\ts_cbranch_scc1 .LLW_%=" : "=&v"(v), "=&s"(t), "+s"(c) : "v"(a), "s"(n), "i"(SL) : "memory", "scc"); }
DI void set_m0(unsigned lds) { asm volatile("s_mov_b32 m0, %0" :: "s"(lds) : "memory"); }
template <int OFF> DI void dma16o(unsigned voff, i32x4 srd, unsigned soff) {
  asm volatile("buffer_load_dwordx4 %0, %1, %2 offen offset:%3 lds" :: "v"(voff), "s"(srd), "s"(soff), "i"(OFF) : "memory"); }
DI void dma16c(unsigned lds, unsigned voff, i32x4 srd, unsigned soff) {
  unsigned keep;
  asm volatile("s_mov_b32 %0, m0\n\ts_mov_b32 m0, %1\n\ts_nop 0\n\tbuffer_load_dwordx4 %2, %3, %4 offen sc1 lds\n\ts_mov_b32 m0, %0"
               : "=&s"(keep) : "s"(lds), "v"(voff), "s"(srd), "s"(soff) : "memory"); }
DI void touch4(unsigned lds, unsigned voff, i32x4 srd) {
  unsigned keep;
  asm volatile("s_mov_b32 %0, m0\n\ts_mov_b32 m0, %1\n\ts_nop 0\n\tbuffer_load_dword %2, %3, 0 offen lds\n\ts_mov_b32 m0, %0"
               : "=&s"(keep) : "s"(lds), "v"(voff), "s"(srd) : "memory"); }
DI int xswz(int t) { return (((t >> 3) & 1) << 2) | ((((t >> 2) ^ (t >> 4)) & 1) << 1) | ((t >> 1) & 1); }
DI int z_u0(int g) { return g < 7 ? 7 * g : 48; }
DI int z_nu(int g) { return g < 6 ? 7 : 6; }
template <int TYPE> struct P1 { static constexpr int NU = TYPE < 2 ? 4 : TYPE < 4 ? 7 : 1, MT = TYPE < 2 ? 4 : 2; };

struct Stage { i32x4 sx, sw; unsigned voff[6], ldx, ldw; u64 ep; };
DI void stage_issue1(const Stage& g, int i, unsigned slot, unsigned ox, unsigned ow) {
  if (i == 0 || i == 2) { set_m0((i ? g.ldw : g.ldx) + slot); asm volatile("s_nop 0"); }
  if (i == 0) dma16o<0>(g.voff[0], g.sx, ox); else if (i == 1) dma16o<1024>(g.voff[1], g.sx, ox);
  else if (i == 2) dma16o<0>(g.voff[2], g.sw, ow); else if (i == 3) dma16o<1024>(g.voff[3], g.sw, ow);
  else if (i == 4) dma16o<2048>(g.voff[4], g.sw, ow); else dma16o<3072>(g.voff[5], g.sw, ow); }
DI void stage_open(Stage& g, const __bf16* x, const __bf16* wqkv, const __bf16* win, char* ws, unsigned lds0, int w, int lane, int rb, int isz, int gi) {
  asm volatile("" : "+s"(x), "+s"(wqkv), "+s"(win), "+s"(ws));
  g.ep = *(const __attribute__((address_space(4))) u64*)(ws + WS_EP);
  const unsigned wstr = 3840 - 384 * isz, wz = (224u * gi) & ~63u, win1 = isz ? wz : 512 * gi, win2 = isz ? win1 + 1728 : gi == 7 ? 0x40000000u : win1 + 256;
  g.sx = mk_srd((const char*)x - 3072, 2048 * 1280 + 3072); g.sw = mk_srd((const char*)(isz ? win : wqkv) - 3072, 640 * wstr + 3072);
  g.ldx = lds0 + w * 2048; g.ldw = lds0 + 16384 + w * 4096;
#pragma unroll
  for (int i = 0; i < 2; ++i) { const int row = 16 * w + 8 * i + (lane >> 3); g.voff[i] = (unsigned)(rb * 128 + row) * 1280u + (unsigned)(((lane & 7) ^ xswz(row & 31)) * 16) + 3072 - 1024 * i; }
  stage_issue1(g, 0, 0, 0, 0); stage_issue1(g, 1, 0, 0, 0);
#pragma unroll
  for (int i = 0; i < 4; ++i) { const int k = 8 * w + 2 * i + (lane >> 5), R = (k & 3) | (((k >> 3) & 1) << 2);
    const int c = (lane & 31) ^ (!isz && gi < 5 ? (R & 3) | ((R >> 2) << 3) : 2 * R);
    g.voff[2 + i] = k * wstr + (c < 16 ? win1 + 16 * c : win2 + 16 * (c - 16)) + 3072 - 1024 * i; }
#pragma unroll
  for (int n = 2; n < 18; ++n) stage_issue1(g, n % 6, (n / 6) * K1_SLOT, (n / 6) * 128, (n / 6) * 64 * wstr);
  SBAR();
}

template <int TYPE>
DI u64 k1_gemm(f32x4 (&acc)[P1<TYPE>::NU][P1<TYPE>::MT], Stage& g, lds_ptr lds, int w, int lane, int row0, int v0, char* ws) {
  constexpr bool Z = TYPE == 2 || TYPE == 3, TOK = TYPE == 1 || TYPE == 3;
  constexpr int NU = P1<TYPE>::NU, MT = P1<TYPE>::MT, NM = NU * MT, NR = TYPE == 4 ? 3 : MT + 2 * NU;
  constexpr int NP = 6, NS = 3;
  constexpr unsigned SLOT = K1_SLOT, WROW = 512, WSTR = Z ? 3456 : 3840;
  unsigned ox = 0, ow = 0;
  auto aim = [&](int u) { ox = u * 128; ow = u * (64 * WSTR); };
  auto issue1 = [&](int i, unsigned slot) { stage_issue1(g, i, slot, ox, ow); };
  const int i16 = lane & 15, kg = lane >> 4;
  unsigned xa[2], wa[NU];
#pragma unroll
  for (int m = 0; m < 2; ++m) { const int tk = TOK ? 8 * (i16 >> 2) + 4 * m + (i16 & 3) : 16 * m + i16; const int r = row0 + tk;
    xa[m] = (r >> 3) * 1024 + (r & 7) * 128 + ((kg ^ xswz(tk)) * 16); }
  { const int q = i16 >> 2, p = i16 & 3, R = q | ((kg & 1) << 2), sw = TYPE == 0 ? 2 * ((R & 3) | ((R >> 2) << 3)) : 4 * R;
#pragma unroll
    for (int j = 0; j < NU; ++j) { const int v = v0 + (TYPE == 0 ? 4 * p + (j ^ (p & 1)) : 4 * j + p); wa[j] = 16384 + (8 * kg + q) * WROW + 8 * (v ^ sw); } }
  bf16x8 fx[2][MT], fg[2]; s16x4 fw[2][NU][2];
  auto rd1 = [&](int b, unsigned slot, int ks, int r, int st) {
    if (r < MT) fx[b][r] = *(const LAS bf16x8*)(lds + slot + (xa[r & 1] ^ (ks * 64)) + (r >> 1) * 4096);
    else if (TYPE == 4) fg[b] = *(const LAS bf16x8*)(lds + L_GW + st * 128 + ks * 64 + kg * 16);
    else fw[b][(r - MT) >> 1][(r - MT) & 1] = __builtin_amdgcn_ds_read_tr16_b64_v4i16((LAS s16x4*)(lds + slot + wa[(r - MT) >> 1] + ks * (32 * WROW) + ((r - MT) & 1) * (4 * WROW))); };
  auto mma1 = [&](int b, int i) { const int j = i / MT, m = i % MT;
    const bf16x8 wf = TYPE == 4 ? fg[b] : __builtin_bit_cast(bf16x8, __builtin_shufflevector(fw[b][j][0], fw[b][j][1], 0, 1, 2, 3, 4, 5, 6, 7));
    acc[j][m] = (TOK || TYPE == 4) ? mfma16(fx[b][m], wf, acc[j][m]) : mfma16(wf, fx[b][m], acc[j][m]); };
  auto half = [&](int bc, unsigned rslot, int ks, int st, bool dma, unsigned dslot) {
    if (TYPE == 4) {
#pragma unroll
      for (int r = 0; r < NR; ++r) rd1(bc ^ 1, rslot, ks, r, st);
      mma1(bc, 0); mma1(bc, 1); SBAR();
      if (dma) {
#pragma unroll
        for (int i = 0; i < NP; ++i) issue1(i, dslot); }
      return; }
#pragma unroll
    for (int i = 0; i < NM; ++i) {
      constexpr int SL = NM > 3 ? NM - 3 : 1;
      mma1(bc, i); SBAR();
#pragma unroll
      for (int r = (i * NR + SL - 1) / SL; r < ((i + 1) * NR + SL - 1) / SL; ++r) if (r < NR) rd1(bc ^ 1, rslot, ks, r, st);
      SBAR();
      if (dma && i >= 2 && !(i & 1) && i / 2 <= NP) { issue1(i / 2 - 1, dslot); SBAR(); }
    } };
#pragma unroll
  for (int j = 0; j < NU; ++j)
#pragma unroll
    for (int m = 0; m < MT; ++m) acc[j][m] = f32x4{};
  if (w == 0) {
#pragma unroll
    for (int i = 0; i < 4; ++i) *(LAS u32x4*)(lds + L_CNT + 16 * i) = u32x4{0u, 0u, 0u, 0u}; }
  asm volatile("s_waitcnt vmcnt(%1) lgkmcnt(0)\n\ts_barrier" : "+s"(g.ep) : "i"(TYPE == 4 ? 0 : (NS - 1) * NP) : "memory");
  const u64 tag = next_tag(g.ep);
  unsigned cur = 0;
  auto next = [&](unsigned c) { return c == (NS - 1) * SLOT ? 0 : c + SLOT; };
#pragma unroll
  for (int r = 0; r < NR; ++r) rd1(0, cur, 0, r, 0);
  SBAR();
#pragma unroll
  for (int s = 0; s < 9; ++s) {
    const unsigned nxt = next(cur);
    half(0, cur, 1, s, false, 0);
    if (s < 8) asm volatile("s_waitcnt vmcnt(%0) lgkmcnt(0)\n\ts_barrier" :: "i"((NS - 2) * NP) : "memory");
    else asm volatile("s_waitcnt vmcnt(0) lgkmcnt(0)\n\ts_barrier" ::: "memory");
    aim(s + NS);
    half(1, nxt, 0, s + 1, s + NS < 10, cur);
    cur = nxt;
  }
  half(0, cur, 1, 9, false, 0);
  asm volatile("s_waitcnt vmcnt(0) lgkmcnt(0)\n\ts_barrier" ::: "memory");
#pragma unroll
  for (int i = 0; i < NM; ++i) mma1(1, i);
  return tag;
}

DI void wring_prime(unsigned ring, i32x4 srw, unsigned sw, unsigned vw0, unsigned vw1) {
#pragma unroll
  for (int n = 0; n < 16; n += 4) { const unsigned so = sw + (n >> 3) * 128 + (n & 7) * 32768;
    set_m0(ring + n * 1024); asm volatile("s_nop 0");
    dma16o<0>(vw0, srw, so); dma16o<1024>(vw1, srw, so + 32768 - 1024); dma16o<2048>(vw0, srw, so + 2 * 32768 - 2048); dma16o<3072>(vw1, srw, so + 3 * 32768 - 3072); }
}

DI u64 proj_phase(const __bf16* x, const __bf16* wqkv, const __bf16* win, const __bf16* gatew,
                  char* ws, lds_ptr lds, unsigned lds0, int lane, int w) {
  const int b = blockIdx.x, xcd = b & 7, bi = b >> 3;
  const int rb = (xcd >> 2) * 8 + (bi >> 2), isz = bi & 1, g = 2 * (xcd & 3) + ((bi >> 1) & 1);
  const rsrc_t rws = mk_rsrc(ws, WS_END);
  if (w == 7) {
    u64 pc; unsigned nb;
    asm volatile("s_getpc_b64 %0\n.LLcs_%=:\n\ts_mov_b32 %1, .LLce_%=-.LLcs_%=\n\t.subsection 1\n.LLce_%=:\n\t.subsection 0" : "=s"(pc), "=s"(nb));
    touch4(lds0 + L_GW + 2048, lane < 24 ? (bi + 32 * lane) * 128 : 0x7fff0000u, mk_srd((const void*)(pc & ~127ull), nb)); }
  Stage sg; stage_open(sg, x, wqkv, win, ws, lds0, w, lane, rb, isz, g);
  const int i16 = lane & 15, fq = lane >> 4;
  u64 tag;
  if (!isz) {
    const int wm = w >> 2, L = 4 * g + (w & 3), T0 = rb * 4 + 2 * wm;
    if (g < 5) {
      f32x4 acc[4][4];
      tag = k1_gemm<0>(acc, sg, lds, w, lane, 64 * wm, 16 * (w & 3), ws);
      const bool isq = L < 10; const float sc = isq ? 0.32259476f : 1.0f;
#pragma unroll
      for (int pp = 0; pp < 2; ++pp) { const int pi = 8 * (isq ? L : L - 10) + 2 * fq + pp, hp = pi / 5, r = pi - 5 * hp;
        const unsigned base = (isq ? WS_Q : WS_K) + ((T0 * 16 + hp) * 5 + r) * 512 + i16 * 16;
#pragma unroll
        for (int m = 0; m < 4; ++m) { const u32x2 a = pk4(acc[2 * pp][m] * sc), c = pk4(acc[2 * pp + 1][m] * sc);
          st16((fq & 1) ? u32x4{c[0], c[1], a[0], a[1]} : u32x4{a[0], a[1], c[0], c[1]}, rws, base + (m >> 1) * 40960 + (m & 1) * 256); } }
    } else if (L < 30) {
      f32x4 acc[4][4];
      tag = k1_gemm<1>(acc, sg, lds, w, lane, 64 * wm, 16 * (w & 3), ws);
#pragma unroll
      for (int j = 0; j < 4; ++j)
#pragma unroll
        for (int mm = 0; mm < 2; ++mm) st16(pk8(acc[j][2 * mm], acc[j][2 * mm + 1]), rws, WS_V + (((T0 + mm) * 4 + fq) * 640 + 64 * (L - 20) + 16 * j + i16) * 16);
    } else {
      if (w == 2) { const i32x4 srg = mk_srd(gatew, 1280); dma16(lds0 + L_GW, lane * 16, srg, 0); dma16(lds0 + L_GW + 1024, 1024 + lane * 16, srg, 0); }
      f32x4 acc[1][2]; const int t4 = 2 * wm + (w & 1);
      tag = k1_gemm<4>(acc, sg, lds, w, lane, 32 * t4, 0, ws);
#pragma unroll
      for (int m = 0; m < 2; ++m) { u32x4 o;
#pragma unroll
        for (int r = 0; r < 4; ++r) o[r] = __float_as_uint(__builtin_amdgcn_rcpf(1.0f + __builtin_amdgcn_exp2f(-1.44269504f * acc[0][m][r])));
        if (i16 == 0) st16(o, rws, WS_GATE + (128 * rb + 32 * t4 + 16 * m + 4 * fq) * 4); }
    }
    publish_n(ws, WS_FQ + (g * 16 + rb) * 128, tag, lane, lds0 + L_CNT + 24, 8);
    return tag;
  }
  const int mq = w & 3, T = rb * 4 + mq;
  const int u0 = z_u0(g), nu = z_nu(g);
  f32x4 acc[7][2];
  if (w < 4) {
    tag = k1_gemm<2>(acc, sg, lds, w, lane, 32 * mq, 4 * (u0 & 1), ws);
    __builtin_amdgcn_s_setprio(2);
    const lds_ptr st = lds + L_WR + mq * 24576 + 16384 + lane * 16; const int o = u0 & 1;
#pragma unroll
    for (int m = 0; m < 2; ++m) {
#pragma unroll
      for (int p = 0; p < 3; ++p) *(LAS u32x4*)(st + (2 * p + m) * 1024) = o ? pk8(acc[2 * p + 1][m], acc[2 * p + 2][m]) : pk8(acc[2 * p][m], acc[2 * p + 1][m]);
      *(LAS u32x2*)(st + (6 + m) * 1024) = o ? pk4(acc[0][m]) : pk4(acc[6][m]); }
    if (lane == 0) asm volatile("s_waitcnt vmcnt(0) lgkmcnt(0)\n\tds_add_u32 %0, %1" :: "v"(lds0 + L_CNT + 24), "v"(1u) : "memory");

    return tag;
  }
  tag = k1_gemm<3>(acc, sg, lds, w, lane, 32 * mq, 32 + 4 * (u0 & 1), ws);
  __builtin_amdgcn_s_setprio(1); SBAR();
  f32x4 sm[2], sq[2];
#pragma unroll
  for (int m = 0; m < 2; ++m) {
#pragma unroll
    for (int j = 0; j < 7; ++j) { acc[j][m] = gelu4(acc[j][m]); asm volatile("" : "+v"(acc[j][m])); }
    sm[m] = acc[0][m]; sq[m] = acc[0][m] * acc[0][m];
#pragma unroll
    for (int j = 1; j < 6; ++j) { sm[m] += acc[j][m]; sq[m] = __builtin_elementwise_fma(acc[j][m], acc[j][m], sq[m]); }
    if (nu == 7) { sm[m] += acc[6][m]; sq[m] = __builtin_elementwise_fma(acc[6][m], acc[6][m], sq[m]); } }
  {
    const bool b0 = i16 & 1, b1 = i16 & 2, b2 = i16 & 4; float s1[4], q1[4], s2[2], q2[2];
#pragma unroll
    for (int k = 0; k < 4; ++k) { const int e = 2 * k; const float sa = sm[e >> 2][e & 3], sb = sm[e >> 2][(e & 3) + 1], qa = sq[e >> 2][e & 3], qb = sq[e >> 2][(e & 3) + 1];
      s1[k] = (b0 ? sb : sa) + dppf<0xb1>(b0 ? sa : sb); q1[k] = (b0 ? qb : qa) + dppf<0xb1>(b0 ? qa : qb); }
#pragma unroll
    for (int k = 0; k < 2; ++k) { s2[k] = (b1 ? s1[2 * k + 1] : s1[2 * k]) + dppf<0x4e>(b1 ? s1[2 * k] : s1[2 * k + 1]); q2[k] = (b1 ? q1[2 * k + 1] : q1[2 * k]) + dppf<0x4e>(b1 ? q1[2 * k] : q1[2 * k + 1]); }
    const float st = b2 ? s2[0] : s2[1], qt = b2 ? q2[0] : q2[1];
    float a = (b2 ? s2[1] : s2[0]) + dppf<0x124>(st), c = (b2 ? q2[1] : q2[0]) + dppf<0x124>(qt);
    a += dppf<0x128>(a); c += dppf<0x128>(c);
    const u32x4 gr = {__float_as_uint(a), __float_as_uint(c), __float_as_uint(a) ^ (unsigned)tag, __float_as_uint(c) ^ (unsigned)(tag >> 32)};
    if (i16 < 8) st16(gr, rws, WS_ST + ((T * 8 + g) * 32 + 8 * fq + i16) * 16); }
  auto z1_gelu = [&]() {
    lds_wait4(lds0 + L_CNT + 24); const lds_ptr st = lds + L_WR + mq * 24576 + 16384 + lane * 16;
#pragma unroll
    for (int i = 0; i < 6; ++i) { const u32x4 v = *(const LAS u32x4*)(st + i * 1024);
      *(LAS u32x4*)(st + i * 1024) = pk8(gelu4(f32x4{bflo(v[0]), bfhi(v[0]), bflo(v[1]), bfhi(v[1])}), gelu4(f32x4{bflo(v[2]), bfhi(v[2]), bflo(v[3]), bfhi(v[3])})); }
#pragma unroll
    for (int i = 6; i < 8; ++i) { const u32x2 v = *(const LAS u32x2*)(st + i * 1024); *(LAS u32x2*)(st + i * 1024) = pk4(gelu4(f32x4{bflo(v[0]), bfhi(v[0]), bflo(v[1]), bfhi(v[1])})); } };
  z1_gelu();
  const i32x4 srdws = mk_srd(ws, WS_END);
  u32x4 gr[4]; const unsigned ga = WS_ST + ((T * 8 + 4 * (i16 >> 3)) * 32 + 8 * fq + (i16 & 7)) * 16;
#pragma nounroll
  for (unsigned spins = 0; spins < SPIN_CAP; ++spins) { bool ok = true;
    asm volatile("buffer_load_dwordx4 %0, %4, %5, 0 offen sc1\n\tbuffer_load_dwordx4 %1, %4, %5, 0 offen offset:512 sc1\n\t"
                 "buffer_load_dwordx4 %2, %4, %5, 0 offen offset:1024 sc1\n\tbuffer_load_dwordx4 %3, %4, %5, 0 offen offset:1536 sc1\n\ts_waitcnt vmcnt(0)"
                 : "=&v"(gr[0]), "=&v"(gr[1]), "=&v"(gr[2]), "=&v"(gr[3]) : "v"(ga), "s"(srdws) : "memory");
#pragma unroll
    for (int i = 0; i < 4; ++i) ok &= (gr[i][0] ^ gr[i][2]) == (unsigned)tag && (gr[i][1] ^ gr[i][3]) == (unsigned)(tag >> 32);
    if (__all(ok)) break;
    __builtin_amdgcn_s_sleep(1); }
  float mu, rs;
  { float s = (__uint_as_float(gr[0][0]) + __uint_as_float(gr[1][0])) + (__uint_as_float(gr[2][0]) + __uint_as_float(gr[3][0]));
    float q = (__uint_as_float(gr[0][1]) + __uint_as_float(gr[1][1])) + (__uint_as_float(gr[2][1]) + __uint_as_float(gr[3][1]));
    s += dppf<0x128>(s); q += dppf<0x128>(q); mu = s * (1.0f / 864.0f);
    rs = __builtin_amdgcn_rsqf(fmaxf(q * (1.0f / 864.0f) - mu * mu, 0.f) + 1e-5f); mu = -mu * rs; }
  const f32x4 sa0 = {dppf<0x150>(rs), dppf<0x151>(rs), dppf<0x152>(rs), dppf<0x153>(rs)}, sa1 = {dppf<0x154>(rs), dppf<0x155>(rs), dppf<0x156>(rs), dppf<0x157>(rs)};
  const f32x4 sb0 = {dppf<0x150>(mu), dppf<0x151>(mu), dppf<0x152>(mu), dppf<0x153>(mu)}, sb1 = {dppf<0x154>(mu), dppf<0x155>(mu), dppf<0x156>(mu), dppf<0x157>(mu)};
#pragma unroll
  for (int j = 0; j < 7; ++j)
    if (j < 6 || nu == 7) st16(pk8(__builtin_elementwise_fma(acc[j][0], sa0, sb0), __builtin_elementwise_fma(acc[j][1], sa1, sb1)), rws, WS_ZT + (T * 54 + u0 + j) * 1024 + lane * 16);
  publish_n(ws, WS_FZT + (g * 16 + rb) * 128, tag, lane, lds0 + L_CNT + 28, 4);
  return tag;
}

DI void wf_task(int task, const __bf16* wo, const __bf16* wout, lds_ptr lds, unsigned lds0, unsigned ring, int lane, u32x4 (&d)[6]) {
  const int i16 = lane & 15, fq = lane >> 4, ks = task >> 3, gg = task & 7; const unsigned area = ring + 16384;
  const i32x4 srs = ks < 27 ? mk_srd(wout, 864 * 1280) : mk_srd(wo, 640 * 1280);
#pragma unroll
  for (int i = 0; i < 5; ++i) { const int p = 64 * i + lane, rho = p / 10, c = p - 10 * rho, kq = rho >> 3, e = rho & 7; int k0, k1;
    if (ks < 27) { k0 = 32 * ks + 4 * kq; k1 = k0 + 16; }
    else { const int sg = 4 * (ks - 27) + kq; if (sg < 64) { k0 = 20 * (sg >> 1) + 4 * (sg & 1); k1 = k0 + 8; } else { k0 = 40 * (sg - 64) + 16; k1 = k0 + 20; } }
    dma16(area + i * 1024, (e < 4 ? k0 + e : k1 + e - 4) * 1280 + gg * 160 + c * 16, srs, 0); }
  asm volatile("s_waitcnt vmcnt(0)" ::: "memory");
  const lds_ptr ta = lds + (area - lds0) + (8 * fq + (i16 >> 2)) * 160 + 40 * (i16 & 3);
#pragma unroll
  for (int j = 0; j < 5; ++j) {
    const s16x4 lo = __builtin_amdgcn_ds_read_tr16_b64_v4i16((LAS s16x4*)(ta + 8 * j)), hi = __builtin_amdgcn_ds_read_tr16_b64_v4i16((LAS s16x4*)(ta + 8 * j + 640));
    d[j] = __builtin_bit_cast(u32x4, __builtin_shufflevector(lo, hi, 0, 1, 2, 3, 4, 5, 6, 7)); }
}
DI f32x16 attn_safe(rsrc_t rws, unsigned qoff, unsigned koff, unsigned voff, unsigned vk, unsigned vk4, unsigned vv, int lane) {
  const bf16x8 q16 = ldfrag(rws, vk, qoff); u32x2 q4 = __builtin_amdgcn_raw_buffer_load_b64(rws, vk4 + 1024, qoff, AUX_SC1);
  if (lane >= 32) q4 = u32x2{0u, 0u};
  f32x16 o = {}; float mx = -INFINITY; const bool real = (lane & 31) < 20; const u32x4 ones = {0x3f803f80u, 0x3f803f80u, 0x3f803f80u, 0x3f803f80u};
#pragma nounroll
  for (int t = 0; t < 64; ++t) {
    const bf16x8 k16 = ldfrag(rws, vk, koff + t * 40960); const u32x2 k4 = __builtin_amdgcn_raw_buffer_load_b64(rws, vk4 + 1024, koff + t * 40960, AUX_SC1);
    u32x4 v0 = __builtin_amdgcn_raw_buffer_load_b128(rws, vv, voff + t * 40960, AUX_SC1), v1 = __builtin_amdgcn_raw_buffer_load_b128(rws, vv + 20480, voff + t * 40960, AUX_SC1);
    v0 = real ? v0 : ones; v1 = real ? v1 : ones;
    f32x16 s = __builtin_amdgcn_mfma_f32_32x32x16_bf16(k16, q16, f32x16{}, 0, 0, 0);
    s = __builtin_amdgcn_mfma_f32_32x32x8bf16_1k(__builtin_bit_cast(s16x4, k4), __builtin_bit_cast(s16x4, q4), s, 0, 0, 0);
    float tm = mx;
#pragma unroll
    for (int r = 0; r < 16; ++r) tm = fmaxf(tm, s[r]);
    tm = fmaxf(tm, __shfl_xor(tm, 32));
    const float al = __builtin_amdgcn_exp2f(mx - tm); mx = tm; u32x8 p0, p1;
#pragma unroll
    for (int r = 0; r < 8; ++r) { p0[r] = __float_as_uint(__builtin_amdgcn_exp2f(s[r] - tm)); p1[r] = __float_as_uint(__builtin_amdgcn_exp2f(s[8 + r] - tm)); }
    o *= al;
    o = __builtin_amdgcn_smfmac_f32_32x32x32_bf16(__builtin_bit_cast(bf16x8, v0), __builtin_bit_cast(bf16x16, p0), o, (int)0xdddddddd, 0, 0);
    o = __builtin_amdgcn_smfmac_f32_32x32x32_bf16(__builtin_bit_cast(bf16x8, v1), __builtin_bit_cast(bf16x16, p1), o, (int)0xdddddddd, 0, 0);
  }
  return o;
}

__global__ __launch_bounds__(512, 2) void gated_mix(const __bf16* __restrict__ x, const __bf16* __restrict__ wqkv, const __bf16* __restrict__ win,
                                                    const __bf16* __restrict__ wo, const __bf16* __restrict__ wout, const __bf16* __restrict__ gatew,
                                                    const __bf16* __restrict__ wsp, const __bf16* __restrict__ bsp, char* ws, __bf16* out) {
  extern __shared__ __attribute__((aligned(16))) char smem[];
  const lds_ptr lds = (lds_ptr)smem; const unsigned lds0 = (unsigned)(size_t)smem;
  int lane = threadIdx.x & 63; const int wv = rfl(threadIdx.x >> 6), w = wv & 3;
  const int b = blockIdx.x, xcd = b & 7, bi = b >> 3;
  const int qb = bi >> 2, g = 4 * (xcd & 1) + (bi & 3), rb = 4 * qb + (xcd >> 1), head = 8 * (xcd >> 1) + g;
  const unsigned ring = lds0 + L_WR + w * 24576, sw = rb * 64 * 4096 + w * 1024; const i32x4 srw = mk_srd(wsp, 2048 * 4096);
  const u64 tag = proj_phase(x, wqkv, win, gatew, ws, lds, lds0, lane, wv);
  asm volatile("" : "+v"(lane));
  const rsrc_t rws = mk_rsrc(ws, WS_END); const i32x4 srd = mk_srd(ws, WS_END);
  const unsigned cW = lds0 + L_CNT, cB = cW + 36, cX = cW + 8, cF = cW + 12, cA = cW + 16, cS = cW + 20, cU = cW + 48;
  if (wv < 4) {
    const int odd = head & 1, hp = head >> 1;
    const unsigned vk = lane * 16 + odd * 1536, vk4 = (lane & 31) * 16 + odd * 8;
    const unsigned qo = WS_Q + (8 * qb * 16 + hp) * 2560, ko = WS_K + hp * 2560, vo = WS_V + head * 320;
    const int rbo = 4 * qb + w;
    float gt[2];
    f32x16 o[8];
    { unsigned st = (16 * w - 1) * 40960, t, t2, m0s, vq = vk, vq4 = lane < 32 ? vk4 + 1024 : 0x7fff0000u;
      const unsigned ar = lds0 + L_AR + w * 12352; unsigned ds[3], qs[8];
#pragma unroll
      for (int i = 0; i < 3; ++i) { const int p = 64 * i + lane, v = p - 96, tg = v / 20;
        ds[i] = p < 96 ? ko + odd * 1024 + p * 16 - 1024 * i : p < 176 ? vo + tg * 10240 + (v - 20 * tg) * 16 - 1024 * i : 0x7fff0000u; }
#pragma unroll
      for (int j = 0; j < 8; ++j) qs[j] = qo + ((2 * w + j) & 7) * 40960;
      const unsigned axb = lds0 + L_AX - ar - odd * 512;
      const int d = lane & 31;
      unsigned rk = ar + odd * 512 + lane * 16, rk4 = ar + (lane >= 32 ? 2816 : (odd ? 8 : 1024) + d * 16);
      unsigned rv0 = ar + (d < 20 ? 1536 + (lane >> 5) * 320 + d * 16 : d == 20 ? 3072 : 2816), rv1 = rv0 + (d < 20 ? 640 : 0);
      const int p0 = 5 * hp + 2 * odd, e = lane & 1, lq = (p0 + 2 * e) >> 3, lv = 20 + ((20 * head + 19 * e) >> 6);
      unsigned vg = WS_GATE + (64 * rbo + d) * 4;
      unsigned vp = WS_FQ + (lane < 8 ? ((10 + lq) >> 2) * 16 + 4 * w + (lane >> 1) : lane < 16 ? (lv >> 2) * 16 + 4 * w + ((lane - 8) >> 1)
                           : lane < 20 ? (lq >> 2) * 16 + 2 * qb + ((lane - 16) >> 1) : 7 * 16 + (rbo >> 1)) * 128;
      asm volatile(
    "s_nop 4\n\t"
    "s_mov_b32 %[m0s], m0\n\t"
    "v_mov_b32 v180, 0x3f803f80\n\t"
    "v_mov_b32 v181, 0x3f803f80\n\t"
    "v_mov_b32 v182, 0x3f803f80\n\t"
    "v_mov_b32 v183, 0x3f803f80\n\t"
    "v_mov_b32 v184, %[ring]\n\t"
    "ds_write_b128 v184, v[180:183] offset:3072\n\t"
    "ds_write_b128 v184, v[180:183] offset:6160\n\t"
    "ds_write_b128 v184, v[180:183] offset:9248\n\t"
    "ds_write_b128 v184, v[180:183] offset:12336\n\t"
    "v_mov_b32 v228, 0\n\t"
    "v_mov_b32 v229, 0\n\t"
    "v_mov_b32 v230, 0\n\t"
    "v_mov_b32 v231, 0\n\t"
    "v_mov_b32 v232, 0\n\t"
    "v_mov_b32 v233, 0\n\t"
    "v_mov_b32 v234, 0\n\t"
    "v_mov_b32 v235, 0\n\t"
    "s_nop 1\n\t"
    "v_mfma_f32_32x32x16_bf16 v[0:15], v[228:231], v[228:231], 0\n\t"
    "v_mfma_f32_32x32x16_bf16 v[16:31], v[228:231], v[228:231], 0\n\t"
    "v_mfma_f32_32x32x16_bf16 v[32:47], v[228:231], v[228:231], 0\n\t"
    "v_mfma_f32_32x32x16_bf16 v[48:63], v[228:231], v[228:231], 0\n\t"
    "v_mfma_f32_32x32x16_bf16 v[64:79], v[228:231], v[228:231], 0\n\t"
    "v_mfma_f32_32x32x16_bf16 v[80:95], v[228:231], v[228:231], 0\n\t"
    "v_mfma_f32_32x32x16_bf16 v[96:111], v[228:231], v[228:231], 0\n\t"
    "v_mfma_f32_32x32x16_bf16 v[112:127], v[228:231], v[228:231], 0\n\t"
    "v_mfma_f32_32x32x16_bf16 v[192:207], v[228:231], v[228:231], 0\n\t"
    "v_mov_b32 v236, 0xdddddddd\n\t"
    "v_mov_b32 v180, %[tlo]\n\t"
    "v_mov_b32 v181, %[thi]\n\t"
    "s_waitcnt vmcnt(0) lgkmcnt(0)\n\t"
    "s_mov_b32 %[t], 262144\n\t"
    ".LLP_%=:\n\t"
    "buffer_load_dwordx2 v[182:183], v179, %[srd], 0 offen sc1\n\t"
    "s_waitcnt vmcnt(0)\n\t"
    "v_cmp_ne_u64 vcc, v[182:183], v[180:181]\n\t"
    "s_nop 1\n\t"
    "s_and_b64 exec, exec, vcc\n\t"
    "s_cbranch_scc0 .LLQ_%=\n\t"
    "s_sleep 1\n\t"
    "s_sub_u32 %[t], %[t], 1\n\t"
    "s_cmp_lg_u32 %[t], 0\n\t"
    "s_cbranch_scc1 .LLP_%=\n\t"
    ".LLQ_%=:\n\t"
    "s_mov_b64 exec, -1\n\t"
    "s_add_u32 m0, %[ring], 0\n\t"
    "s_add_u32 %[st], %[st], 40960\n\t"
    "buffer_load_dwordx4 v237, %[srd], %[st] offen offset:0 sc1 lds\n\t"
    "buffer_load_dwordx4 v238, %[srd], %[st] offen offset:1024 sc1 lds\n\t"
    "buffer_load_dwordx4 v239, %[srd], %[st] offen offset:2048 sc1 lds\n\t"
    "v_mov_b32 v225, %[cs]\n\t"
    "v_mov_b32 v226, 1\n\t"
    "buffer_load_dwordx4 v[128:131], v223, %[srd], %[q0] offen sc1\n\t"
    "buffer_load_dwordx2 v[160:161], v224, %[srd], %[q0] offen sc1\n\t"
    "buffer_load_dwordx4 v[132:135], v223, %[srd], %[q1] offen sc1\n\t"
    "buffer_load_dwordx2 v[162:163], v224, %[srd], %[q1] offen sc1\n\t"
    "buffer_load_dwordx4 v[136:139], v223, %[srd], %[q2] offen sc1\n\t"
    "buffer_load_dwordx2 v[164:165], v224, %[srd], %[q2] offen sc1\n\t"
    "buffer_load_dwordx4 v[140:143], v223, %[srd], %[q3] offen sc1\n\t"
    "buffer_load_dwordx2 v[166:167], v224, %[srd], %[q3] offen sc1\n\t"
    "buffer_load_dwordx4 v[144:147], v223, %[srd], %[q4] offen sc1\n\t"
    "buffer_load_dwordx2 v[168:169], v224, %[srd], %[q4] offen sc1\n\t"
    "s_waitcnt vmcnt(10)\n\t"
    "ds_read_b128 v[208:211], v240 offset:0\n\t"
    "ds_read_b64 v[212:213], v241 offset:0\n\t"
    "ds_read_b128 v[214:217], v242 offset:0\n\t"
    "ds_read_b128 v[218:221], v243 offset:0\n\t"
    "s_waitcnt vmcnt(8) lgkmcnt(0)\n\t"
    "v_mfma_f32_32x32x16_bf16 v[176:191], v[208:211], v[128:131], 0\n\t"
    "v_mfma_f32_32x32x8bf16_1k v[176:191], v[212:213], v[160:161], v[176:191]\n\t"
    "s_setprio 0\n\t"
    "s_nop 15\n\t"
    "v_smfmac_f32_32x32x32_bf16 v[112:127], v[228:231], v[192:199], v236\n\t"
    "v_exp_f32 v176, v176\n\t"
    "buffer_load_dwordx4 v[148:151], v223, %[srd], %[q5] offen sc1\n\t"
    "v_exp_f32 v177, v177\n\t"
    "buffer_load_dwordx2 v[170:171], v224, %[srd], %[q5] offen sc1\n\t"
    "v_exp_f32 v178, v178\n\t"
    "v_smfmac_f32_32x32x32_bf16 v[112:127], v[232:235], v[200:207], v236\n\t"
    "v_exp_f32 v179, v179\n\t"
    "buffer_load_dwordx4 v[152:155], v223, %[srd], %[q6] offen sc1\n\t"
    "v_exp_f32 v180, v180\n\t"
    "buffer_load_dwordx2 v[172:173], v224, %[srd], %[q6] offen sc1\n\t"
    "v_exp_f32 v181, v181\n\t"
    "s_waitcnt vmcnt(10)\n\t"
    "v_mfma_f32_32x32x16_bf16 v[192:207], v[208:211], v[132:135], 0\n\t"
    "v_exp_f32 v182, v182\n\t"
    "buffer_load_dwordx4 v[156:159], v223, %[srd], %[q7] offen sc1\n\t"
    "v_exp_f32 v183, v183\n\t"
    "buffer_load_dwordx2 v[174:175], v224, %[srd], %[q7] offen sc1\n\t"
    "v_exp_f32 v184, v184\n\t"
    "s_add_u32 m0, %[ring], 3088\n\t"
    "v_exp_f32 v185, v185\n\t"
    "v_mfma_f32_32x32x8bf16_1k v[192:207], v[212:213], v[162:163], v[192:207]\n\t"
    "v_exp_f32 v186, v186\n\t"
    "s_add_u32 %[st], %[st], 40960\n\t"
    "v_exp_f32 v187, v187\n\t"
    "buffer_load_dwordx4 v237, %[srd], %[st] offen offset:0 sc1 lds\n\t"
    "v_exp_f32 v188, v188\n\t"
    "buffer_load_dwordx4 v238, %[srd], %[st] offen offset:1024 sc1 lds\n\t"
    "v_exp_f32 v189, v189\n\t"
    "buffer_load_dwordx4 v239, %[srd], %[st] offen offset:2048 sc1 lds\n\t"
    "v_exp_f32 v190, v190\n\t"
    "v_exp_f32 v191, v191\n\t"
    "v_smfmac_f32_32x32x32_bf16 v[0:15], v[214:217], v[176:183], v236\n\t"
    "v_exp_f32 v192, v192\n\t"
    "s_add_u32 m0, %[ring], 6176\n\t"
    "v_exp_f32 v193, v193\n\t"
    "s_add_u32 %[st], %[st], 40960\n\t"
    "v_exp_f32 v194, v194\n\t"
    "v_smfmac_f32_32x32x32_bf16 v[0:15], v[218:221], v[184:191], v236\n\t"
    "v_exp_f32 v195, v195\n\t"
    "buffer_load_dwordx4 v237, %[srd], %[st] offen offset:0 sc1 lds\n\t"
    "v_exp_f32 v196, v196\n\t"
    "buffer_load_dwordx4 v238, %[srd], %[st] offen offset:1024 sc1 lds\n\t"
    "v_exp_f32 v197, v197\n\t"
    "s_waitcnt vmcnt(15)\n\t"
    "v_mfma_f32_32x32x16_bf16 v[176:191], v[208:211], v[136:139], 0\n\t"
    "v_exp_f32 v198, v198\n\t"
    "buffer_load_dwordx4 v239, %[srd], %[st] offen offset:2048 sc1 lds\n\t"
    "v_exp_f32 v199, v199\n\t"
    "s_add_u32 m0, %[ring], 9264\n\t"
    "v_exp_f32 v200, v200\n\t"
    "s_add_u32 %[st], %[st], 40960\n\t"
    "v_exp_f32 v201, v201\n\t"
    "v_mfma_f32_32x32x8bf16_1k v[176:191], v[212:213], v[164:165], v[176:191]\n\t"
    "v_exp_f32 v202, v202\n\t"
    "buffer_load_dwordx4 v237, %[srd], %[st] offen offset:0 sc1 lds\n\t"
    "v_exp_f32 v203, v203\n\t"
    "buffer_load_dwordx4 v238, %[srd], %[st] offen offset:1024 sc1 lds\n\t"
    "v_exp_f32 v204, v204\n\t"
    "buffer_load_dwordx4 v239, %[srd], %[st] offen offset:2048 sc1 lds\n\t"
    "v_exp_f32 v205, v205\n\t"
    "v_exp_f32 v206, v206\n\t"
    "v_exp_f32 v207, v207\n\t"
    "v_smfmac_f32_32x32x32_bf16 v[16:31], v[214:217], v[192:199], v236\n\t"
    "v_exp_f32 v176, v176\n\t"
    "s_add_u32 m0, %[ring], 0\n\t"
    "v_exp_f32 v177, v177\n\t"
    "s_add_u32 %[st], %[st], 40960\n\t"
    "v_exp_f32 v178, v178\n\t"
    "v_smfmac_f32_32x32x32_bf16 v[16:31], v[218:221], v[200:207], v236\n\t"
    "v_exp_f32 v179, v179\n\t"
    "buffer_load_dwordx4 v237, %[srd], %[st] offen offset:0 sc1 lds\n\t"
    "v_exp_f32 v180, v180\n\t"
    "buffer_load_dwordx4 v238, %[srd], %[st] offen offset:1024 sc1 lds\n\t"
    "v_exp_f32 v181, v181\n\t"
    "s_waitcnt vmcnt(19)\n\t"
    "v_mfma_f32_32x32x16_bf16 v[192:207], v[208:211], v[140:143], 0\n\t"
    "v_exp_f32 v182, v182\n\t"
    "buffer_load_dwordx4 v239, %[srd], %[st] offen offset:2048 sc1 lds\n\t"
    "v_exp_f32 v183, v183\n\t"
    "buffer_load_dword v244, v222, %[srd], 0 offen sc1\n\t"
    "v_exp_f32 v184, v184\n\t"
    "buffer_load_dword v245, v222, %[srd], 0 offen offset:128 sc1\n\t"
    "v_exp_f32 v185, v185\n\t"
    "v_mfma_f32_32x32x8bf16_1k v[192:207], v[212:213], v[166:167], v[192:207]\n\t"
    "v_exp_f32 v186, v186\n\t"
    "ds_add_u32 v225, v226\n\t"
    "v_exp_f32 v187, v187\n\t"
    "v_exp_f32 v188, v188\n\t"
    "v_exp_f32 v189, v189\n\t"
    "v_exp_f32 v190, v190\n\t"
    "v_exp_f32 v191, v191\n\t"
    "v_smfmac_f32_32x32x32_bf16 v[32:47], v[214:217], v[176:183], v236\n\t"
    "v_exp_f32 v192, v192\n\t"
    "v_exp_f32 v193, v193\n\t"
    "v_exp_f32 v194, v194\n\t"
    "v_smfmac_f32_32x32x32_bf16 v[32:47], v[218:221], v[184:191], v236\n\t"
    "v_exp_f32 v195, v195\n\t"
    "v_exp_f32 v196, v196\n\t"
    "v_exp_f32 v197, v197\n\t"
    "s_waitcnt vmcnt(20)\n\t"
    "v_mfma_f32_32x32x16_bf16 v[176:191], v[208:211], v[144:147], 0\n\t"
    "v_exp_f32 v198, v198\n\t"
    "v_exp_f32 v199, v199\n\t"
    "v_exp_f32 v200, v200\n\t"
    "v_exp_f32 v201, v201\n\t"
    "v_mfma_f32_32x32x8bf16_1k v[176:191], v[212:213], v[168:169], v[176:191]\n\t"
    "v_exp_f32 v202, v202\n\t"
    "v_exp_f32 v203, v203\n\t"
    "v_exp_f32 v204, v204\n\t"
    "v_exp_f32 v205, v205\n\t"
    "v_exp_f32 v206, v206\n\t"
    "v_exp_f32 v207, v207\n\t"
    "v_smfmac_f32_32x32x32_bf16 v[48:63], v[214:217], v[192:199], v236\n\t"
    "v_exp_f32 v176, v176\n\t"
    "s_waitcnt vmcnt(11) lgkmcnt(0)\n\t"
    "v_exp_f32 v177, v177\n\t"
    "ds_read_b128 v[222:225], v240 offset:3088\n\t"
    "v_exp_f32 v178, v178\n\t"
    "v_smfmac_f32_32x32x32_bf16 v[48:63], v[218:221], v[200:207], v236\n\t"
    "v_exp_f32 v179, v179\n\t"
    "ds_read_b64 v[226:227], v241 offset:3088\n\t"
    "v_exp_f32 v180, v180\n\t"
    "ds_read_b128 v[228:231], v242 offset:3088\n\t"
    "v_exp_f32 v181, v181\n\t"
    "s_waitcnt vmcnt(18)\n\t"
    "v_mfma_f32_32x32x16_bf16 v[192:207], v[208:211], v[148:151], 0\n\t"
    "v_exp_f32 v182, v182\n\t"
    "ds_read_b128 v[232:235], v243 offset:3088\n\t"
    "v_exp_f32 v183, v183\n\t"
    "v_exp_f32 v184, v184\n\t"
    "v_exp_f32 v185, v185\n\t"
    "v_mfma_f32_32x32x8bf16_1k v[192:207], v[212:213], v[170:171], v[192:207]\n\t"
    "v_exp_f32 v186, v186\n\t"
    "v_exp_f32 v187, v187\n\t"
    "v_exp_f32 v188, v188\n\t"
    "v_exp_f32 v189, v189\n\t"
    "v_exp_f32 v190, v190\n\t"
    "v_exp_f32 v191, v191\n\t"
    "v_smfmac_f32_32x32x32_bf16 v[64:79], v[214:217], v[176:183], v236\n\t"
    "v_exp_f32 v192, v192\n\t"
    "v_exp_f32 v193, v193\n\t"
    "v_exp_f32 v194, v194\n\t"
    "v_smfmac_f32_32x32x32_bf16 v[64:79], v[218:221], v[184:191], v236\n\t"
    "v_exp_f32 v195, v195\n\t"
    "v_exp_f32 v196, v196\n\t"
    "v_exp_f32 v197, v197\n\t"
    "s_waitcnt vmcnt(16)\n\t"
    "v_mfma_f32_32x32x16_bf16 v[176:191], v[208:211], v[152:155], 0\n\t"
    "v_exp_f32 v198, v198\n\t"
    "v_exp_f32 v199, v199\n\t"
    "v_exp_f32 v200, v200\n\t"
    "v_exp_f32 v201, v201\n\t"
    "v_mfma_f32_32x32x8bf16_1k v[176:191], v[212:213], v[172:173], v[176:191]\n\t"
    "v_exp_f32 v202, v202\n\t"
    "v_exp_f32 v203, v203\n\t"
    "v_exp_f32 v204, v204\n\t"
    "v_exp_f32 v205, v205\n\t"
    "v_exp_f32 v206, v206\n\t"
    "v_exp_f32 v207, v207\n\t"
    "v_smfmac_f32_32x32x32_bf16 v[80:95], v[214:217], v[192:199], v236\n\t"
    "v_exp_f32 v176, v176\n\t"
    "s_waitcnt vmcnt(8) lgkmcnt(0)\n\t"
    "v_exp_f32 v177, v177\n\t"
    "s_add_u32 m0, %[ring], 3088\n\t"
    "v_exp_f32 v178, v178\n\t"
    "v_smfmac_f32_32x32x32_bf16 v[80:95], v[218:221], v[200:207], v236\n\t"
    "v_exp_f32 v179, v179\n\t"
    "s_add_u32 %[st], %[st], 40960\n\t"
    "v_exp_f32 v180, v180\n\t"
    "buffer_load_dwordx4 v237, %[srd], %[st] offen offset:0 sc1 lds\n\t"
    "v_exp_f32 v181, v181\n\t"
    "s_waitcnt vmcnt(15)\n\t"
    "v_mfma_f32_32x32x16_bf16 v[192:207], v[208:211], v[156:159], 0\n\t"
    "v_exp_f32 v182, v182\n\t"
    "buffer_load_dwordx4 v238, %[srd], %[st] offen offset:1024 sc1 lds\n\t"
    "v_exp_f32 v183, v183\n\t"
    "buffer_load_dwordx4 v239, %[srd], %[st] offen offset:2048 sc1 lds\n\t"
    "v_exp_f32 v184, v184\n\t"
    "v_exp_f32 v185, v185\n\t"
    "v_mfma_f32_32x32x8bf16_1k v[192:207], v[212:213], v[174:175], v[192:207]\n\t"
    "v_exp_f32 v186, v186\n\t"
    "v_exp_f32 v187, v187\n\t"
    "v_exp_f32 v188, v188\n\t"
    "v_exp_f32 v189, v189\n\t"
    "v_exp_f32 v190, v190\n\t"
    "v_exp_f32 v191, v191\n\t"
    "v_smfmac_f32_32x32x32_bf16 v[96:111], v[214:217], v[176:183], v236\n\t"
    "v_exp_f32 v192, v192\n\t"
    "v_exp_f32 v193, v193\n\t"
    "v_exp_f32 v194, v194\n\t"
    "v_smfmac_f32_32x32x32_bf16 v[96:111], v[218:221], v[184:191], v236\n\t"
    "v_exp_f32 v195, v195\n\t"
    "v_exp_f32 v196, v196\n\t"
    "v_exp_f32 v197, v197\n\t"
    "v_mfma_f32_32x32x16_bf16 v[176:191], v[222:225], v[128:131], 0\n\t"
    "v_exp_f32 v198, v198\n\t"
    "v_exp_f32 v199, v199\n\t"
    "v_exp_f32 v200, v200\n\t"
    "v_exp_f32 v201, v201\n\t"
    "v_mfma_f32_32x32x8bf16_1k v[176:191], v[226:227], v[160:161], v[176:191]\n\t"
    "v_exp_f32 v202, v202\n\t"
    "v_exp_f32 v203, v203\n\t"
    "v_exp_f32 v204, v204\n\t"
    "v_exp_f32 v205, v205\n\t"
    "v_exp_f32 v206, v206\n\t"
    "v_exp_f32 v207, v207\n\t"
    "v_smfmac_f32_32x32x32_bf16 v[112:127], v[214:217], v[192:199], v236\n\t"
    "v_exp_f32 v176, v176\n\t"
    "v_exp_f32 v177, v177\n\t"
    "v_exp_f32 v178, v178\n\t"
    "v_smfmac_f32_32x32x32_bf16 v[112:127], v[218:221], v[200:207], v236\n\t"
    "v_exp_f32 v179, v179\n\t"
    "v_exp_f32 v180, v180\n\t"
    "v_exp_f32 v181, v181\n\t"
    "v_mfma_f32_32x32x16_bf16 v[192:207], v[222:225], v[132:135], 0\n\t"
    "v_exp_f32 v182, v182\n\t"
    "v_exp_f32 v183, v183\n\t"
    "v_exp_f32 v184, v184\n\t"
    "v_exp_f32 v185, v185\n\t"
    "v_mfma_f32_32x32x8bf16_1k v[192:207], v[226:227], v[162:163], v[192:207]\n\t"
    "v_exp_f32 v186, v186\n\t"
    "v_exp_f32 v187, v187\n\t"
    "v_exp_f32 v188, v188\n\t"
    "v_exp_f32 v189, v189\n\t"
    "v_exp_f32 v190, v190\n\t"
    "v_exp_f32 v191, v191\n\t"
    "v_smfmac_f32_32x32x32_bf16 v[0:15], v[228:231], v[176:183], v236\n\t"
    "v_exp_f32 v192, v192\n\t"
    "ds_read_b128 v[208:211], v240 offset:6176\n\t"
    "v_exp_f32 v193, v193\n\t"
    "ds_read_b64 v[212:213], v241 offset:6176\n\t"
    "v_exp_f32 v194, v194\n\t"
    "v_smfmac_f32_32x32x32_bf16 v[0:15], v[232:235], v[184:191], v236\n\t"
    "v_exp_f32 v195, v195\n\t"
    "ds_read_b128 v[214:217], v242 offset:6176\n\t"
    "v_exp_f32 v196, v196\n\t"
    "ds_read_b128 v[218:221], v243 offset:6176\n\t"
    "v_exp_f32 v197, v197\n\t"
    "v_mfma_f32_32x32x16_bf16 v[176:191], v[222:225], v[136:139], 0\n\t"
    "v_exp_f32 v198, v198\n\t"
    "v_exp_f32 v199, v199\n\t"
    "v_exp_f32 v200, v200\n\t"
    "v_exp_f32 v201, v201\n\t"
    "v_mfma_f32_32x32x8bf16_1k v[176:191], v[226:227], v[164:165], v[176:191]\n\t"
    "v_exp_f32 v202, v202\n\t"
    "v_exp_f32 v203, v203\n\t"
    "v_exp_f32 v204, v204\n\t"
    "v_exp_f32 v205, v205\n\t"
    "v_exp_f32 v206, v206\n\t"
    "v_exp_f32 v207, v207\n\t"
    "v_smfmac_f32_32x32x32_bf16 v[16:31], v[228:231], v[192:199], v236\n\t"
    "v_exp_f32 v176, v176\n\t"
    "v_exp_f32 v177, v177\n\t"
    "v_exp_f32 v178, v178\n\t"
    "v_smfmac_f32_32x32x32_bf16 v[16:31], v[232:235], v[200:207], v236\n\t"
    "v_exp_f32 v179, v179\n\t"
    "v_exp_f32 v180, v180\n\t"
    "v_exp_f32 v181, v181\n\t"
    "v_mfma_f32_32x32x16_bf16 v[192:207], v[222:225], v[140:143], 0\n\t"
    "v_exp_f32 v182, v182\n\t"
    "v_exp_f32 v183, v183\n\t"
    "v_exp_f32 v184, v184\n\t"
    "v_exp_f32 v185, v185\n\t"
    "v_mfma_f32_32x32x8bf16_1k v[192:207], v[226:227], v[166:167], v[192:207]\n\t"
    "v_exp_f32 v186, v186\n\t"
    "v_exp_f32 v187, v187\n\t"
    "v_exp_f32 v188, v188\n\t"
    "v_exp_f32 v189, v189\n\t"
    "v_exp_f32 v190, v190\n\t"
    "v_exp_f32 v191, v191\n\t"
    "v_smfmac_f32_32x32x32_bf16 v[32:47], v[228:231], v[176:183], v236\n\t"
    "v_exp_f32 v192, v192\n\t"
    "v_exp_f32 v193, v193\n\t"
    "v_exp_f32 v194, v194\n\t"
    "v_smfmac_f32_32x32x32_bf16 v[32:47], v[232:235], v[184:191], v236\n\t"
    "v_exp_f32 v195, v195\n\t"
    "v_exp_f32 v196, v196\n\t"
    "v_exp_f32 v197, v197\n\t"
    "v_mfma_f32_32x32x16_bf16 v[176:191], v[222:225], v[144:147], 0\n\t"
    "v_exp_f32 v198, v198\n\t"
    "v_exp_f32 v199, v199\n\t"
    "v_exp_f32 v200, v200\n\t"
    "v_exp_f32 v201, v201\n\t"
    "v_mfma_f32_32x32x8bf16_1k v[176:191], v[226:227], v[168:169], v[176:191]\n\t"
    "v_exp_f32 v202, v202\n\t"
    "v_exp_f32 v203, v203\n\t"
    "v_exp_f32 v204, v204\n\t"
    "v_exp_f32 v205, v205\n\t"
    "v_exp_f32 v206, v206\n\t"
    "v_exp_f32 v207, v207\n\t"
    "v_smfmac_f32_32x32x32_bf16 v[48:63], v[228:231], v[192:199], v236\n\t"
    "v_exp_f32 v176, v176\n\t"
    "v_exp_f32 v177, v177\n\t"
    "v_exp_f32 v178, v178\n\t"
    "v_smfmac_f32_32x32x32_bf16 v[48:63], v[232:235], v[200:207], v236\n\t"
    "v_exp_f32 v179, v179\n\t"
    "v_exp_f32 v180, v180\n\t"
    "v_exp_f32 v181, v181\n\t"
    "v_mfma_f32_32x32x16_bf16 v[192:207], v[222:225], v[148:151], 0\n\t"
    "v_exp_f32 v182, v182\n\t"
    "v_exp_f32 v183, v183\n\t"
    "v_exp_f32 v184, v184\n\t"
    "v_exp_f32 v185, v185\n\t"
    "v_mfma_f32_32x32x8bf16_1k v[192:207], v[226:227], v[170:171], v[192:207]\n\t"
    "v_exp_f32 v186, v186\n\t"
    "v_exp_f32 v187, v187\n\t"
    "v_exp_f32 v188, v188\n\t"
    "v_exp_f32 v189, v189\n\t"
    "v_exp_f32 v190, v190\n\t"
    "v_exp_f32 v191, v191\n\t"
    "v_smfmac_f32_32x32x32_bf16 v[64:79], v[228:231], v[176:183], v236\n\t"
    "v_exp_f32 v192, v192\n\t"
    "s_waitcnt vmcnt(8) lgkmcnt(0)\n\t"
    "v_exp_f32 v193, v193\n\t"
    "s_add_u32 m0, %[ring], 6176\n\t"
    "v_exp_f32 v194, v194\n\t"
    "v_smfmac_f32_32x32x32_bf16 v[64:79], v[232:235], v[184:191], v236\n\t"
    "v_exp_f32 v195, v195\n\t"
    "s_add_u32 %[st], %[st], 40960\n\t"
    "v_exp_f32 v196, v196\n\t"
    "buffer_load_dwordx4 v237, %[srd], %[st] offen offset:0 sc1 lds\n\t"
    "v_exp_f32 v197, v197\n\t"
    "v_mfma_f32_32x32x16_bf16 v[176:191], v[222:225], v[152:155], 0\n\t"
    "v_exp_f32 v198, v198\n\t"
    "buffer_load_dwordx4 v238, %[srd], %[st] offen offset:1024 sc1 lds\n\t"
    "v_exp_f32 v199, v199\n\t"
    "buffer_load_dwordx4 v239, %[srd], %[st] offen offset:2048 sc1 lds\n\t"
    "v_exp_f32 v200, v200\n\t"
    "v_exp_f32 v201, v201\n\t"
    "v_mfma_f32_32x32x8bf16_1k v[176:191], v[226:227], v[172:173], v[176:191]\n\t"
    "v_exp_f32 v202, v202\n\t"
    "v_exp_f32 v203, v203\n\t"
    "v_exp_f32 v204, v204\n\t"
    "v_exp_f32 v205, v205\n\t"
    "v_exp_f32 v206, v206\n\t"
    "v_exp_f32 v207, v207\n\t"
    "v_smfmac_f32_32x32x32_bf16 v[80:95], v[228:231], v[192:199], v236\n\t"
    "v_exp_f32 v176, v176\n\t"
    "v_exp_f32 v177, v177\n\t"
    "v_exp_f32 v178, v178\n\t"
    "v_smfmac_f32_32x32x32_bf16 v[80:95], v[232:235], v[200:207], v236\n\t"
    "v_exp_f32 v179, v179\n\t"
    "v_exp_f32 v180, v180\n\t"
    "v_exp_f32 v181, v181\n\t"
    "v_mfma_f32_32x32x16_bf16 v[192:207], v[222:225], v[156:159], 0\n\t"
    "v_exp_f32 v182, v182\n\t"
    "v_exp_f32 v183, v183\n\t"
    "v_exp_f32 v184, v184\n\t"
    "v_exp_f32 v185, v185\n\t"
    "v_mfma_f32_32x32x8bf16_1k v[192:207], v[226:227], v[174:175], v[192:207]\n\t"
    "v_exp_f32 v186, v186\n\t"
    "v_exp_f32 v187, v187\n\t"
    "v_exp_f32 v188, v188\n\t"
    "v_exp_f32 v189, v189\n\t"
    "v_exp_f32 v190, v190\n\t"
    "v_exp_f32 v191, v191\n\t"
    "v_smfmac_f32_32x32x32_bf16 v[96:111], v[228:231], v[176:183], v236\n\t"
    "v_exp_f32 v192, v192\n\t"
    "v_exp_f32 v193, v193\n\t"
    "v_exp_f32 v194, v194\n\t"
    "v_smfmac_f32_32x32x32_bf16 v[96:111], v[232:235], v[184:191], v236\n\t"
    "v_exp_f32 v195, v195\n\t"
    "v_exp_f32 v196, v196\n\t"
    "v_exp_f32 v197, v197\n\t"
    "v_mfma_f32_32x32x16_bf16 v[176:191], v[208:211], v[128:131], 0\n\t"
    "v_exp_f32 v198, v198\n\t"
    "v_exp_f32 v199, v199\n\t"
    "v_exp_f32 v200, v200\n\t"
    "v_exp_f32 v201, v201\n\t"
    "v_mfma_f32_32x32x8bf16_1k v[176:191], v[212:213], v[160:161], v[176:191]\n\t"
    "v_exp_f32 v202, v202\n\t"
    "v_exp_f32 v203, v203\n\t"
    "v_exp_f32 v204, v204\n\t"
    "v_exp_f32 v205, v205\n\t"
    "v_exp_f32 v206, v206\n\t"
    "v_exp_f32 v207, v207\n\t"
    "v_smfmac_f32_32x32x32_bf16 v[112:127], v[228:231], v[192:199], v236\n\t"
    "v_exp_f32 v176, v176\n\t"
    "v_exp_f32 v177, v177\n\t"
    "v_exp_f32 v178, v178\n\t"
    "v_smfmac_f32_32x32x32_bf16 v[112:127], v[232:235], v[200:207], v236\n\t"
    "v_exp_f32 v179, v179\n\t"
    "v_exp_f32 v180, v180\n\t"
    "v_exp_f32 v181, v181\n\t"
    "v_mfma_f32_32x32x16_bf16 v[192:207], v[208:211], v[132:135], 0\n\t"
    "v_exp_f32 v182, v182\n\t"
    "v_exp_f32 v183, v183\n\t"
    "v_exp_f32 v184, v184\n\t"
    "v_exp_f32 v185, v185\n\t"
    "v_mfma_f32_32x32x8bf16_1k v[192:207], v[212:213], v[162:163], v[192:207]\n\t"
    "v_exp_f32 v186, v186\n\t"
    "v_exp_f32 v187, v187\n\t"
    "v_exp_f32 v188, v188\n\t"
    "v_exp_f32 v189, v189\n\t"
    "v_exp_f32 v190, v190\n\t"
    "v_exp_f32 v191, v191\n\t"
    "v_smfmac_f32_32x32x32_bf16 v[0:15], v[214:217], v[176:183], v236\n\t"
    "v_exp_f32 v192, v192\n\t"
    "ds_read_b128 v[222:225], v240 offset:9264\n\t"
    "v_exp_f32 v193, v193\n\t"
    "ds_read_b64 v[226:227], v241 offset:9264\n\t"
    "v_exp_f32 v194, v194\n\t"
    "v_smfmac_f32_32x32x32_bf16 v[0:15], v[218:221], v[184:191], v236\n\t"
    "v_exp_f32 v195, v195\n\t"
    "ds_read_b128 v[228:231], v242 offset:9264\n\t"
    "v_exp_f32 v196, v196\n\t"
    "ds_read_b128 v[232:235], v243 offset:9264\n\t"
    "v_exp_f32 v197, v197\n\t"
    "v_mfma_f32_32x32x16_bf16 v[176:191], v[208:211], v[136:139], 0\n\t"
    "v_exp_f32 v198, v198\n\t"
    "v_exp_f32 v199, v199\n\t"
    "v_exp_f32 v200, v200\n\t"
    "v_exp_f32 v201, v201\n\t"
    "v_mfma_f32_32x32x8bf16_1k v[176:191], v[212:213], v[164:165], v[176:191]\n\t"
    "v_exp_f32 v202, v202\n\t"
    "v_exp_f32 v203, v203\n\t"
    "v_exp_f32 v204, v204\n\t"
    "v_exp_f32 v205, v205\n\t"
    "v_exp_f32 v206, v206\n\t"
    "v_exp_f32 v207, v207\n\t"
    "v_smfmac_f32_32x32x32_bf16 v[16:31], v[214:217], v[192:199], v236\n\t"
    "v_exp_f32 v176, v176\n\t"
    "v_exp_f32 v177, v177\n\t"
    "v_exp_f32 v178, v178\n\t"
    "v_smfmac_f32_32x32x32_bf16 v[16:31], v[218:221], v[200:207], v236\n\t"
    "v_exp_f32 v179, v179\n\t"
    "v_exp_f32 v180, v180\n\t"
    "v_exp_f32 v181, v181\n\t"
    "v_mfma_f32_32x32x16_bf16 v[192:207], v[208:211], v[140:143], 0\n\t"
    "v_exp_f32 v182, v182\n\t"
    "v_exp_f32 v183, v183\n\t"
    "v_exp_f32 v184, v184\n\t"
    "v_exp_f32 v185, v185\n\t"
    "v_mfma_f32_32x32x8bf16_1k v[192:207], v[212:213], v[166:167], v[192:207]\n\t"
    "v_exp_f32 v186, v186\n\t"
    "v_exp_f32 v187, v187\n\t"
    "v_exp_f32 v188, v188\n\t"
    "v_exp_f32 v189, v189\n\t"
    "v_exp_f32 v190, v190\n\t"
    "v_exp_f32 v191, v191\n\t"
    "v_smfmac_f32_32x32x32_bf16 v[32:47], v[214:217], v[176:183], v236\n\t"
    "v_exp_f32 v192, v192\n\t"
    "v_exp_f32 v193, v193\n\t"
    "v_exp_f32 v194, v194\n\t"
    "v_smfmac_f32_32x32x32_bf16 v[32:47], v[218:221], v[184:191], v236\n\t"
    "v_exp_f32 v195, v195\n\t"
    "v_exp_f32 v196, v196\n\t"
    "v_exp_f32 v197, v197\n\t"
    "v_mfma_f32_32x32x16_bf16 v[176:191], v[208:211], v[144:147], 0\n\t"
    "v_exp_f32 v198, v198\n\t"
    "v_exp_f32 v199, v199\n\t"
    "v_exp_f32 v200, v200\n\t"
    "v_exp_f32 v201, v201\n\t"
    "v_mfma_f32_32x32x8bf16_1k v[176:191], v[212:213], v[168:169], v[176:191]\n\t"
    "v_exp_f32 v202, v202\n\t"
    "v_exp_f32 v203, v203\n\t"
    "v_exp_f32 v204, v204\n\t"
    "v_exp_f32 v205, v205\n\t"
    "v_exp_f32 v206, v206\n\t"
    "v_exp_f32 v207, v207\n\t"
    "v_smfmac_f32_32x32x32_bf16 v[48:63], v[214:217], v[192:199], v236\n\t"
    "v_exp_f32 v176, v176\n\t"
    "v_exp_f32 v177, v177\n\t"
    "v_exp_f32 v178, v178\n\t"
    "v_smfmac_f32_32x32x32_bf16 v[48:63], v[218:221], v[200:207], v236\n\t"
    "v_exp_f32 v179, v179\n\t"
    "v_exp_f32 v180, v180\n\t"
    "v_exp_f32 v181, v181\n\t"
    "v_mfma_f32_32x32x16_bf16 v[192:207], v[208:211], v[148:151], 0\n\t"
    "v_exp_f32 v182, v182\n\t"
    "v_exp_f32 v183, v183\n\t"
    "v_exp_f32 v184, v184\n\t"
    "v_exp_f32 v185, v185\n\t"
    "v_mfma_f32_32x32x8bf16_1k v[192:207], v[212:213], v[170:171], v[192:207]\n\t"
    "v_exp_f32 v186, v186\n\t"
    "v_exp_f32 v187, v187\n\t"
    "v_exp_f32 v188, v188\n\t"
    "v_exp_f32 v189, v189\n\t"
    "v_exp_f32 v190, v190\n\t"
    "v_exp_f32 v191, v191\n\t"
    "v_smfmac_f32_32x32x32_bf16 v[64:79], v[214:217], v[176:183], v236\n\t"
    "v_exp_f32 v192, v192\n\t"
    "s_waitcnt vmcnt(8) lgkmcnt(0)\n\t"
    "v_exp_f32 v193, v193\n\t"
    "s_add_u32 m0, %[ring], 9264\n\t"
    "v_exp_f32 v194, v194\n\t"
    "v_smfmac_f32_32x32x32_bf16 v[64:79], v[218:221], v[184:191], v236\n\t"
    "v_exp_f32 v195, v195\n\t"
    "s_add_u32 %[st], %[st], 40960\n\t"
    "v_exp_f32 v196, v196\n\t"
    "buffer_load_dwordx4 v237, %[srd], %[st] offen offset:0 sc1 lds\n\t"
    "v_exp_f32 v197, v197\n\t"
    "v_mfma_f32_32x32x16_bf16 v[176:191], v[208:211], v[152:155], 0\n\t"
    "v_exp_f32 v198, v198\n\t"
    "buffer_load_dwordx4 v238, %[srd], %[st] offen offset:1024 sc1 lds\n\t"
    "v_exp_f32 v199, v199\n\t"
    "buffer_load_dwordx4 v239, %[srd], %[st] offen offset:2048 sc1 lds\n\t"
    "v_exp_f32 v200, v200\n\t"
    "v_exp_f32 v201, v201\n\t"
    "v_mfma_f32_32x32x8bf16_1k v[176:191], v[212:213], v[172:173], v[176:191]\n\t"
    "v_exp_f32 v202, v202\n\t"
    "v_exp_f32 v203, v203\n\t"
    "v_exp_f32 v204, v204\n\t"
    "v_exp_f32 v205, v205\n\t"
    "v_exp_f32 v206, v206\n\t"
    "v_exp_f32 v207, v207\n\t"
    "v_smfmac_f32_32x32x32_bf16 v[80:95], v[214:217], v[192:199], v236\n\t"
    "v_exp_f32 v176, v176\n\t"
    "v_exp_f32 v177, v177\n\t"
    "v_exp_f32 v178, v178\n\t"
    "v_smfmac_f32_32x32x32_bf16 v[80:95], v[218:221], v[200:207], v236\n\t"
    "v_exp_f32 v179, v179\n\t"
    "v_exp_f32 v180, v180\n\t"
    "v_exp_f32 v181, v181\n\t"
    "v_mfma_f32_32x32x16_bf16 v[192:207], v[208:211], v[156:159], 0\n\t"
    "v_exp_f32 v182, v182\n\t"
    "v_exp_f32 v183, v183\n\t"
    "v_exp_f32 v184, v184\n\t"
    "v_exp_f32 v185, v185\n\t"
    "v_mfma_f32_32x32x8bf16_1k v[192:207], v[212:213], v[174:175], v[192:207]\n\t"
    "v_exp_f32 v186, v186\n\t"
    "v_exp_f32 v187, v187\n\t"
    "v_exp_f32 v188, v188\n\t"
    "v_exp_f32 v189, v189\n\t"
    "v_exp_f32 v190, v190\n\t"
    "v_exp_f32 v191, v191\n\t"
    "v_smfmac_f32_32x32x32_bf16 v[96:111], v[214:217], v[176:183], v236\n\t"
    "v_exp_f32 v192, v192\n\t"
    "v_exp_f32 v193, v193\n\t"
    "v_exp_f32 v194, v194\n\t"
    "v_smfmac_f32_32x32x32_bf16 v[96:111], v[218:221], v[184:191], v236\n\t"
    "v_exp_f32 v195, v195\n\t"
    "v_exp_f32 v196, v196\n\t"
    "v_exp_f32 v197, v197\n\t"
    "v_mfma_f32_32x32x16_bf16 v[176:191], v[222:225], v[128:131], 0\n\t"
    "v_exp_f32 v198, v198\n\t"
    "v_exp_f32 v199, v199\n\t"
    "v_exp_f32 v200, v200\n\t"
    "v_exp_f32 v201, v201\n\t"
    "v_mfma_f32_32x32x8bf16_1k v[176:191], v[226:227], v[160:161], v[176:191]\n\t"
    "v_exp_f32 v202, v202\n\t"
    "v_exp_f32 v203, v203\n\t"
    "v_exp_f32 v204, v204\n\t"
    "v_exp_f32 v205, v205\n\t"
    "v_exp_f32 v206, v206\n\t"
    "v_exp_f32 v207, v207\n\t"
    "v_smfmac_f32_32x32x32_bf16 v[112:127], v[214:217], v[192:199], v236\n\t"
    "v_exp_f32 v176, v176\n\t"
    "v_exp_f32 v177, v177\n\t"
    "v_exp_f32 v178, v178\n\t"
    "v_smfmac_f32_32x32x32_bf16 v[112:127], v[218:221], v[200:207], v236\n\t"
    "v_exp_f32 v179, v179\n\t"
    "v_exp_f32 v180, v180\n\t"
    "v_exp_f32 v181, v181\n\t"
    "v_mfma_f32_32x32x16_bf16 v[192:207], v[222:225], v[132:135], 0\n\t"
    "v_exp_f32 v182, v182\n\t"
    "v_exp_f32 v183, v183\n\t"
    "v_exp_f32 v184, v184\n\t"
    "v_exp_f32 v185, v185\n\t"
    "v_mfma_f32_32x32x8bf16_1k v[192:207], v[226:227], v[162:163], v[192:207]\n\t"
    "v_exp_f32 v186, v186\n\t"
    "v_exp_f32 v187, v187\n\t"
    "v_exp_f32 v188, v188\n\t"
    "v_exp_f32 v189, v189\n\t"
    "v_exp_f32 v190, v190\n\t"
    "v_exp_f32 v191, v191\n\t"
    "v_smfmac_f32_32x32x32_bf16 v[0:15], v[228:231], v[176:183], v236\n\t"
    "v_exp_f32 v192, v192\n\t"
    "ds_read_b128 v[208:211], v240 offset:0\n\t"
    "v_exp_f32 v193, v193\n\t"
    "ds_read_b64 v[212:213], v241 offset:0\n\t"
    "v_exp_f32 v194, v194\n\t"
    "v_smfmac_f32_32x32x32_bf16 v[0:15], v[232:235], v[184:191], v236\n\t"
    "v_exp_f32 v195, v195\n\t"
    "ds_read_b128 v[214:217], v242 offset:0\n\t"
    "v_exp_f32 v196, v196\n\t"
    "ds_read_b128 v[218:221], v243 offset:0\n\t"
    "v_exp_f32 v197, v197\n\t"
    "v_mfma_f32_32x32x16_bf16 v[176:191], v[222:225], v[136:139], 0\n\t"
    "v_exp_f32 v198, v198\n\t"
    "v_exp_f32 v199, v199\n\t"
    "v_exp_f32 v200, v200\n\t"
    "v_exp_f32 v201, v201\n\t"
    "v_mfma_f32_32x32x8bf16_1k v[176:191], v[226:227], v[164:165], v[176:191]\n\t"
    "v_exp_f32 v202, v202\n\t"
    "v_exp_f32 v203, v203\n\t"
    "v_exp_f32 v204, v204\n\t"
    "v_exp_f32 v205, v205\n\t"
    "v_exp_f32 v206, v206\n\t"
    "v_exp_f32 v207, v207\n\t"
    "v_smfmac_f32_32x32x32_bf16 v[16:31], v[228:231], v[192:199], v236\n\t"
    "v_exp_f32 v176, v176\n\t"
    "v_exp_f32 v177, v177\n\t"
    "v_exp_f32 v178, v178\n\t"
    "v_smfmac_f32_32x32x32_bf16 v[16:31], v[232:235], v[200:207], v236\n\t"
    "v_exp_f32 v179, v179\n\t"
    "v_exp_f32 v180, v180\n\t"
    "v_exp_f32 v181, v181\n\t"
    "v_mfma_f32_32x32x16_bf16 v[192:207], v[222:225], v[140:143], 0\n\t"
    "v_exp_f32 v182, v182\n\t"
    "v_exp_f32 v183, v183\n\t"
    "v_exp_f32 v184, v184\n\t"
    "v_exp_f32 v185, v185\n\t"
    "v_mfma_f32_32x32x8bf16_1k v[192:207], v[226:227], v[166:167], v[192:207]\n\t"
    "v_exp_f32 v186, v186\n\t"
    "v_exp_f32 v187, v187\n\t"
    "v_exp_f32 v188, v188\n\t"
    "v_exp_f32 v189, v189\n\t"
    "v_exp_f32 v190, v190\n\t"
    "v_exp_f32 v191, v191\n\t"
    "v_smfmac_f32_32x32x32_bf16 v[32:47], v[228:231], v[176:183], v236\n\t"
    "v_exp_f32 v192, v192\n\t"
    "v_exp_f32 v193, v193\n\t"
    "v_exp_f32 v194, v194\n\t"
    "v_smfmac_f32_32x32x32_bf16 v[32:47], v[232:235], v[184:191], v236\n\t"
    "v_exp_f32 v195, v195\n\t"
    "v_exp_f32 v196, v196\n\t"
    "v_exp_f32 v197, v197\n\t"
    "v_mfma_f32_32x32x16_bf16 v[176:191], v[222:225], v[144:147], 0\n\t"
    "v_exp_f32 v198, v198\n\t"
    "v_exp_f32 v199, v199\n\t"
    "v_exp_f32 v200, v200\n\t"
    "v_exp_f32 v201, v201\n\t"
    "v_mfma_f32_32x32x8bf16_1k v[176:191], v[226:227], v[168:169], v[176:191]\n\t"
    "v_exp_f32 v202, v202\n\t"
    "v_exp_f32 v203, v203\n\t"
    "v_exp_f32 v204, v204\n\t"
    "v_exp_f32 v205, v205\n\t"
    "v_exp_f32 v206, v206\n\t"
    "v_exp_f32 v207, v207\n\t"
    "v_smfmac_f32_32x32x32_bf16 v[48:63], v[228:231], v[192:199], v236\n\t"
    "v_exp_f32 v176, v176\n\t"
    "v_exp_f32 v177, v177\n\t"
    "v_exp_f32 v178, v178\n\t"
    "v_smfmac_f32_32x32x32_bf16 v[48:63], v[232:235], v[200:207], v236\n\t"
    "v_exp_f32 v179, v179\n\t"
    "v_exp_f32 v180, v180\n\t"
    "v_exp_f32 v181, v181\n\t"
    "v_mfma_f32_32x32x16_bf16 v[192:207], v[222:225], v[148:151], 0\n\t"
    "v_exp_f32 v182, v182\n\t"
    "v_exp_f32 v183, v183\n\t"
    "v_exp_f32 v184, v184\n\t"
    "v_exp_f32 v185, v185\n\t"
    "v_mfma_f32_32x32x8bf16_1k v[192:207], v[226:227], v[170:171], v[192:207]\n\t"
    "v_exp_f32 v186, v186\n\t"
    "v_exp_f32 v187, v187\n\t"
    "v_exp_f32 v188, v188\n\t"
    "v_exp_f32 v189, v189\n\t"
    "v_exp_f32 v190, v190\n\t"
    "v_exp_f32 v191, v191\n\t"
    "v_smfmac_f32_32x32x32_bf16 v[64:79], v[228:231], v[176:183], v236\n\t"
    "v_exp_f32 v192, v192\n\t"
    "s_waitcnt vmcnt(6) lgkmcnt(0)\n\t"
    "v_exp_f32 v193, v193\n\t"
    "s_add_u32 m0, %[ring], 0\n\t"
    "v_exp_f32 v194, v194\n\t"
    "v_smfmac_f32_32x32x32_bf16 v[64:79], v[232:235], v[184:191], v236\n\t"
    "v_exp_f32 v195, v195\n\t"
    "s_add_u32 %[st], %[st], 40960\n\t"
    "v_exp_f32 v196, v196\n\t"
    "buffer_load_dwordx4 v237, %[srd], %[st] offen offset:0 sc1 lds\n\t"
    "v_exp_f32 v197, v197\n\t"
    "v_mfma_f32_32x32x16_bf16 v[176:191], v[222:225], v[152:155], 0\n\t"
    "v_exp_f32 v198, v198\n\t"
    "buffer_load_dwordx4 v238, %[srd], %[st] offen offset:1024 sc1 lds\n\t"
    "v_exp_f32 v199, v199\n\t"
    "buffer_load_dwordx4 v239, %[srd], %[st] offen offset:2048 sc1 lds\n\t"
    "v_exp_f32 v200, v200\n\t"
    "v_exp_f32 v201, v201\n\t"
    "v_mfma_f32_32x32x8bf16_1k v[176:191], v[226:227], v[172:173], v[176:191]\n\t"
    "v_exp_f32 v202, v202\n\t"
    "v_exp_f32 v203, v203\n\t"
    "v_exp_f32 v204, v204\n\t"
    "v_exp_f32 v205, v205\n\t"
    "v_exp_f32 v206, v206\n\t"
    "v_exp_f32 v207, v207\n\t"
    "v_smfmac_f32_32x32x32_bf16 v[80:95], v[228:231], v[192:199], v236\n\t"
    "v_exp_f32 v176, v176\n\t"
    "v_exp_f32 v177, v177\n\t"
    "v_exp_f32 v178, v178\n\t"
    "v_smfmac_f32_32x32x32_bf16 v[80:95], v[232:235], v[200:207], v236\n\t"
    "v_exp_f32 v179, v179\n\t"
    "v_exp_f32 v180, v180\n\t"
    "v_exp_f32 v181, v181\n\t"
    "v_mfma_f32_32x32x16_bf16 v[192:207], v[222:225], v[156:159], 0\n\t"
    "v_exp_f32 v182, v182\n\t"
    "v_exp_f32 v183, v183\n\t"
    "v_exp_f32 v184, v184\n\t"
    "v_exp_f32 v185, v185\n\t"
    "v_mfma_f32_32x32x8bf16_1k v[192:207], v[226:227], v[174:175], v[192:207]\n\t"
    "v_exp_f32 v186, v186\n\t"
    "v_exp_f32 v187, v187\n\t"
    "v_exp_f32 v188, v188\n\t"
    "v_exp_f32 v189, v189\n\t"
    "v_exp_f32 v190, v190\n\t"
    "v_exp_f32 v191, v191\n\t"
    "v_smfmac_f32_32x32x32_bf16 v[96:111], v[228:231], v[176:183], v236\n\t"
    "v_exp_f32 v192, v192\n\t"
    "v_exp_f32 v193, v193\n\t"
    "v_exp_f32 v194, v194\n\t"
    "v_smfmac_f32_32x32x32_bf16 v[96:111], v[232:235], v[184:191], v236\n\t"
    "v_exp_f32 v195, v195\n\t"
    "v_exp_f32 v196, v196\n\t"
    "v_exp_f32 v197, v197\n\t"
    "v_mfma_f32_32x32x16_bf16 v[176:191], v[208:211], v[128:131], 0\n\t"
    "v_exp_f32 v198, v198\n\t"
    "v_exp_f32 v199, v199\n\t"
    "v_exp_f32 v200, v200\n\t"
    "v_exp_f32 v201, v201\n\t"
    "v_mfma_f32_32x32x8bf16_1k v[176:191], v[212:213], v[160:161], v[176:191]\n\t"
    "v_exp_f32 v202, v202\n\t"
    "v_exp_f32 v203, v203\n\t"
    "v_exp_f32 v204, v204\n\t"
    "v_exp_f32 v205, v205\n\t"
    "v_exp_f32 v206, v206\n\t"
    "v_exp_f32 v207, v207\n\t"
    "s_mov_b32 %[t], 2\n\t"
    ".p2align 6\n\t"
    ".LLA_%=:\n\t"
    "v_smfmac_f32_32x32x32_bf16 v[112:127], v[228:231], v[192:199], v236\n\t"
    "v_exp_f32 v176, v176\n\t"
    "v_exp_f32 v177, v177\n\t"
    "v_exp_f32 v178, v178\n\t"
    "v_smfmac_f32_32x32x32_bf16 v[112:127], v[232:235], v[200:207], v236\n\t"
    "v_exp_f32 v179, v179\n\t"
    "v_exp_f32 v180, v180\n\t"
    "v_exp_f32 v181, v181\n\t"
    "v_mfma_f32_32x32x16_bf16 v[192:207], v[208:211], v[132:135], 0\n\t"
    "v_exp_f32 v182, v182\n\t"
    "v_exp_f32 v183, v183\n\t"
    "v_exp_f32 v184, v184\n\t"
    "v_exp_f32 v185, v185\n\t"
    "v_mfma_f32_32x32x8bf16_1k v[192:207], v[212:213], v[162:163], v[192:207]\n\t"
    "v_exp_f32 v186, v186\n\t"
    "v_exp_f32 v187, v187\n\t"
    "v_exp_f32 v188, v188\n\t"
    "v_exp_f32 v189, v189\n\t"
    "v_exp_f32 v190, v190\n\t"
    "v_exp_f32 v191, v191\n\t"
    "v_smfmac_f32_32x32x32_bf16 v[0:15], v[214:217], v[176:183], v236\n\t"
    "v_exp_f32 v192, v192\n\t"
    "ds_read_b128 v[222:225], v240 offset:3088\n\t"
    "v_exp_f32 v193, v193\n\t"
    "ds_read_b64 v[226:227], v241 offset:3088\n\t"
    "v_exp_f32 v194, v194\n\t"
    "v_smfmac_f32_32x32x32_bf16 v[0:15], v[218:221], v[184:191], v236\n\t"
    "v_exp_f32 v195, v195\n\t"
    "ds_read_b128 v[228:231], v242 offset:3088\n\t"
    "v_exp_f32 v196, v196\n\t"
    "ds_read_b128 v[232:235], v243 offset:3088\n\t"
    "v_exp_f32 v197, v197\n\t"
    "v_mfma_f32_32x32x16_bf16 v[176:191], v[208:211], v[136:139], 0\n\t"
    "v_exp_f32 v198, v198\n\t"
    "v_exp_f32 v199, v199\n\t"
    "v_exp_f32 v200, v200\n\t"
    "v_exp_f32 v201, v201\n\t"
    "v_mfma_f32_32x32x8bf16_1k v[176:191], v[212:213], v[164:165], v[176:191]\n\t"
    "v_exp_f32 v202, v202\n\t"
    "v_exp_f32 v203, v203\n\t"
    "v_exp_f32 v204, v204\n\t"
    "v_exp_f32 v205, v205\n\t"
    "v_exp_f32 v206, v206\n\t"
    "v_exp_f32 v207, v207\n\t"
    "v_smfmac_f32_32x32x32_bf16 v[16:31], v[214:217], v[192:199], v236\n\t"
    "v_exp_f32 v176, v176\n\t"
    "v_exp_f32 v177, v177\n\t"
    "v_exp_f32 v178, v178\n\t"
    "v_smfmac_f32_32x32x32_bf16 v[16:31], v[218:221], v[200:207], v236\n\t"
    "v_exp_f32 v179, v179\n\t"
    "v_exp_f32 v180, v180\n\t"
    "v_exp_f32 v181, v181\n\t"
    "v_mfma_f32_32x32x16_bf16 v[192:207], v[208:211], v[140:143], 0\n\t"
    "v_exp_f32 v182, v182\n\t"
    "v_exp_f32 v183, v183\n\t"
    "v_exp_f32 v184, v184\n\t"
    "v_exp_f32 v185, v185\n\t"
    "v_mfma_f32_32x32x8bf16_1k v[192:207], v[212:213], v[166:167], v[192:207]\n\t"
    "v_exp_f32 v186, v186\n\t"
    "v_exp_f32 v187, v187\n\t"
    "v_exp_f32 v188, v188\n\t"
    "v_exp_f32 v189, v189\n\t"
    "v_exp_f32 v190, v190\n\t"
    "v_exp_f32 v191, v191\n\t"
    "v_smfmac_f32_32x32x32_bf16 v[32:47], v[214:217], v[176:183], v236\n\t"
    "v_exp_f32 v192, v192\n\t"
    "v_exp_f32 v193, v193\n\t"
    "v_exp_f32 v194, v194\n\t"
    "v_smfmac_f32_32x32x32_bf16 v[32:47], v[218:221], v[184:191], v236\n\t"
    "v_exp_f32 v195, v195\n\t"
    "v_exp_f32 v196, v196\n\t"
    "v_exp_f32 v197, v197\n\t"
    "v_mfma_f32_32x32x16_bf16 v[176:191], v[208:211], v[144:147], 0\n\t"
    "v_exp_f32 v198, v198\n\t"
    "v_exp_f32 v199, v199\n\t"
    "v_exp_f32 v200, v200\n\t"
    "v_exp_f32 v201, v201\n\t"
    "v_mfma_f32_32x32x8bf16_1k v[176:191], v[212:213], v[168:169], v[176:191]\n\t"
    "v_exp_f32 v202, v202\n\t"
    "v_exp_f32 v203, v203\n\t"
    "v_exp_f32 v204, v204\n\t"
    "v_exp_f32 v205, v205\n\t"
    "v_exp_f32 v206, v206\n\t"
    "v_exp_f32 v207, v207\n\t"
    "v_smfmac_f32_32x32x32_bf16 v[48:63], v[214:217], v[192:199], v236\n\t"
    "v_exp_f32 v176, v176\n\t"
    "v_exp_f32 v177, v177\n\t"
    "v_exp_f32 v178, v178\n\t"
    "v_smfmac_f32_32x32x32_bf16 v[48:63], v[218:221], v[200:207], v236\n\t"
    "v_exp_f32 v179, v179\n\t"
    "v_exp_f32 v180, v180\n\t"
    "v_exp_f32 v181, v181\n\t"
    "v_mfma_f32_32x32x16_bf16 v[192:207], v[208:211], v[148:151], 0\n\t"
    "v_exp_f32 v182, v182\n\t"
    "v_exp_f32 v183, v183\n\t"
    "v_exp_f32 v184, v184\n\t"
    "v_exp_f32 v185, v185\n\t"
    "v_mfma_f32_32x32x8bf16_1k v[192:207], v[212:213], v[170:171], v[192:207]\n\t"
    "v_exp_f32 v186, v186\n\t"
    "v_exp_f32 v187, v187\n\t"
    "v_exp_f32 v188, v188\n\t"
    "v_exp_f32 v189, v189\n\t"
    "v_exp_f32 v190, v190\n\t"
    "v_exp_f32 v191, v191\n\t"
    "v_smfmac_f32_32x32x32_bf16 v[64:79], v[214:217], v[176:183], v236\n\t"
    "v_exp_f32 v192, v192\n\t"
    "s_waitcnt vmcnt(6) lgkmcnt(0)\n\t"
    "v_exp_f32 v193, v193\n\t"
    "s_add_u32 m0, %[ring], 3088\n\t"
    "v_exp_f32 v194, v194\n\t"
    "v_smfmac_f32_32x32x32_bf16 v[64:79], v[218:221], v[184:191], v236\n\t"
    "v_exp_f32 v195, v195\n\t"
    "s_add_u32 %[st], %[st], 40960\n\t"
    "v_exp_f32 v196, v196\n\t"
    "buffer_load_dwordx4 v237, %[srd], %[st] offen offset:0 sc1 lds\n\t"
    "v_exp_f32 v197, v197\n\t"
    "v_mfma_f32_32x32x16_bf16 v[176:191], v[208:211], v[152:155], 0\n\t"
    "v_exp_f32 v198, v198\n\t"
    "buffer_load_dwordx4 v238, %[srd], %[st] offen offset:1024 sc1 lds\n\t"
    "v_exp_f32 v199, v199\n\t"
    "buffer_load_dwordx4 v239, %[srd], %[st] offen offset:2048 sc1 lds\n\t"
    "v_exp_f32 v200, v200\n\t"
    "v_exp_f32 v201, v201\n\t"
    "v_mfma_f32_32x32x8bf16_1k v[176:191], v[212:213], v[172:173], v[176:191]\n\t"
    "v_exp_f32 v202, v202\n\t"
    "v_exp_f32 v203, v203\n\t"
    "v_exp_f32 v204, v204\n\t"
    "v_exp_f32 v205, v205\n\t"
    "v_exp_f32 v206, v206\n\t"
    "v_exp_f32 v207, v207\n\t"
    "v_smfmac_f32_32x32x32_bf16 v[80:95], v[214:217], v[192:199], v236\n\t"
    "v_exp_f32 v176, v176\n\t"
    "v_exp_f32 v177, v177\n\t"
    "v_exp_f32 v178, v178\n\t"
    "v_smfmac_f32_32x32x32_bf16 v[80:95], v[218:221], v[200:207], v236\n\t"
    "v_exp_f32 v179, v179\n\t"
    "v_exp_f32 v180, v180\n\t"
    "v_exp_f32 v181, v181\n\t"
    "v_mfma_f32_32x32x16_bf16 v[192:207], v[208:211], v[156:159], 0\n\t"
    "v_exp_f32 v182, v182\n\t"
    "v_exp_f32 v183, v183\n\t"
    "v_exp_f32 v184, v184\n\t"
    "v_exp_f32 v185, v185\n\t"
    "v_mfma_f32_32x32x8bf16_1k v[192:207], v[212:213], v[174:175], v[192:207]\n\t"
    "v_exp_f32 v186, v186\n\t"
    "v_exp_f32 v187, v187\n\t"
    "v_exp_f32 v188, v188\n\t"
    "v_exp_f32 v189, v189\n\t"
    "v_exp_f32 v190, v190\n\t"
    "v_exp_f32 v191, v191\n\t"
    "v_smfmac_f32_32x32x32_bf16 v[96:111], v[214:217], v[176:183], v236\n\t"
    "v_exp_f32 v192, v192\n\t"
    "v_exp_f32 v193, v193\n\t"
    "v_exp_f32 v194, v194\n\t"
    "v_smfmac_f32_32x32x32_bf16 v[96:111], v[218:221], v[184:191], v236\n\t"
    "v_exp_f32 v195, v195\n\t"
    "v_exp_f32 v196, v196\n\t"
    "v_exp_f32 v197, v197\n\t"
    "v_mfma_f32_32x32x16_bf16 v[176:191], v[222:225], v[128:131], 0\n\t"
    "v_exp_f32 v198, v198\n\t"
    "v_exp_f32 v199, v199\n\t"
    "v_exp_f32 v200, v200\n\t"
    "v_exp_f32 v201, v201\n\t"
    "v_mfma_f32_32x32x8bf16_1k v[176:191], v[226:227], v[160:161], v[176:191]\n\t"
    "v_exp_f32 v202, v202\n\t"
    "v_exp_f32 v203, v203\n\t"
    "v_exp_f32 v204, v204\n\t"
    "v_exp_f32 v205, v205\n\t"
    "v_exp_f32 v206, v206\n\t"
    "v_exp_f32 v207, v207\n\t"
    "v_smfmac_f32_32x32x32_bf16 v[112:127], v[214:217], v[192:199], v236\n\t"
    "v_exp_f32 v176, v176\n\t"
    "v_exp_f32 v177, v177\n\t"
    "v_exp_f32 v178, v178\n\t"
    "v_smfmac_f32_32x32x32_bf16 v[112:127], v[218:221], v[200:207], v236\n\t"
    "v_exp_f32 v179, v179\n\t"
    "v_exp_f32 v180, v180\n\t"
    "v_exp_f32 v181, v181\n\t"
    "v_mfma_f32_32x32x16_bf16 v[192:207], v[222:225], v[132:135], 0\n\t"
    "v_exp_f32 v182, v182\n\t"
    "v_exp_f32 v183, v183\n\t"
    "v_exp_f32 v184, v184\n\t"
    "v_exp_f32 v185, v185\n\t"
    "v_mfma_f32_32x32x8bf16_1k v[192:207], v[226:227], v[162:163], v[192:207]\n\t"
    "v_exp_f32 v186, v186\n\t"
    "v_exp_f32 v187, v187\n\t"
    "v_exp_f32 v188, v188\n\t"
    "v_exp_f32 v189, v189\n\t"
    "v_exp_f32 v190, v190\n\t"
    "v_exp_f32 v191, v191\n\t"
    "v_smfmac_f32_32x32x32_bf16 v[0:15], v[228:231], v[176:183], v236\n\t"
    "v_exp_f32 v192, v192\n\t"
    "ds_read_b128 v[208:211], v240 offset:6176\n\t"
    "v_exp_f32 v193, v193\n\t"
    "ds_read_b64 v[212:213], v241 offset:6176\n\t"
    "v_exp_f32 v194, v194\n\t"
    "v_smfmac_f32_32x32x32_bf16 v[0:15], v[232:235], v[184:191], v236\n\t"
    "v_exp_f32 v195, v195\n\t"
    "ds_read_b128 v[214:217], v242 offset:6176\n\t"
    "v_exp_f32 v196, v196\n\t"
    "ds_read_b128 v[218:221], v243 offset:6176\n\t"
    "v_exp_f32 v197, v197\n\t"
    "v_mfma_f32_32x32x16_bf16 v[176:191], v[222:225], v[136:139], 0\n\t"
    "v_exp_f32 v198, v198\n\t"
    "v_exp_f32 v199, v199\n\t"
    "v_exp_f32 v200, v200\n\t"
    "v_exp_f32 v201, v201\n\t"
    "v_mfma_f32_32x32x8bf16_1k v[176:191], v[226:227], v[164:165], v[176:191]\n\t"
    "v_exp_f32 v202, v202\n\t"
    "v_exp_f32 v203, v203\n\t"
    "v_exp_f32 v204, v204\n\t"
    "v_exp_f32 v205, v205\n\t"
    "v_exp_f32 v206, v206\n\t"
    "v_exp_f32 v207, v207\n\t"
    "v_smfmac_f32_32x32x32_bf16 v[16:31], v[228:231], v[192:199], v236\n\t"
    "v_exp_f32 v176, v176\n\t"
    "v_exp_f32 v177, v177\n\t"
    "v_exp_f32 v178, v178\n\t"
    "v_smfmac_f32_32x32x32_bf16 v[16:31], v[232:235], v[200:207], v236\n\t"
    "v_exp_f32 v179, v179\n\t"
    "v_exp_f32 v180, v180\n\t"
    "v_exp_f32 v181, v181\n\t"
    "v_mfma_f32_32x32x16_bf16 v[192:207], v[222:225], v[140:143], 0\n\t"
    "v_exp_f32 v182, v182\n\t"
    "v_exp_f32 v183, v183\n\t"
    "v_exp_f32 v184, v184\n\t"
    "v_exp_f32 v185, v185\n\t"
    "v_mfma_f32_32x32x8bf16_1k v[192:207], v[226:227], v[166:167], v[192:207]\n\t"
    "v_exp_f32 v186, v186\n\t"
    "v_exp_f32 v187, v187\n\t"
    "v_exp_f32 v188, v188\n\t"
    "v_exp_f32 v189, v189\n\t"
    "v_exp_f32 v190, v190\n\t"
    "v_exp_f32 v191, v191\n\t"
    "v_smfmac_f32_32x32x32_bf16 v[32:47], v[228:231], v[176:183], v236\n\t"
    "v_exp_f32 v192, v192\n\t"
    "v_exp_f32 v193, v193\n\t"
    "v_exp_f32 v194, v194\n\t"
    "v_smfmac_f32_32x32x32_bf16 v[32:47], v[232:235], v[184:191], v236\n\t"
    "v_exp_f32 v195, v195\n\t"
    "v_exp_f32 v196, v196\n\t"
    "v_exp_f32 v197, v197\n\t"
    "v_mfma_f32_32x32x16_bf16 v[176:191], v[222:225], v[144:147], 0\n\t"
    "v_exp_f32 v198, v198\n\t"
    "v_exp_f32 v199, v199\n\t"
    "v_exp_f32 v200, v200\n\t"
    "v_exp_f32 v201, v201\n\t"
    "v_mfma_f32_32x32x8bf16_1k v[176:191], v[226:227], v[168:169], v[176:191]\n\t"
    "v_exp_f32 v202, v202\n\t"
    "v_exp_f32 v203, v203\n\t"
    "v_exp_f32 v204, v204\n\t"
    "v_exp_f32 v205, v205\n\t"
    "v_exp_f32 v206, v206\n\t"
    "v_exp_f32 v207, v207\n\t"
    "v_smfmac_f32_32x32x32_bf16 v[48:63], v[228:231], v[192:199], v236\n\t"
    "v_exp_f32 v176, v176\n\t"
    "v_exp_f32 v177, v177\n\t"
    "v_exp_f32 v178, v178\n\t"
    "v_smfmac_f32_32x32x32_bf16 v[48:63], v[232:235], v[200:207], v236\n\t"
    "v_exp_f32 v179, v179\n\t"
    "v_exp_f32 v180, v180\n\t"
    "v_exp_f32 v181, v181\n\t"
    "v_mfma_f32_32x32x16_bf16 v[192:207], v[222:225], v[148:151], 0\n\t"
    "v_exp_f32 v182, v182\n\t"
    "v_exp_f32 v183, v183\n\t"
    "v_exp_f32 v184, v184\n\t"
    "v_exp_f32 v185, v185\n\t"
    "v_mfma_f32_32x32x8bf16_1k v[192:207], v[226:227], v[170:171], v[192:207]\n\t"
    "v_exp_f32 v186, v186\n\t"
    "v_exp_f32 v187, v187\n\t"
    "v_exp_f32 v188, v188\n\t"
    "v_exp_f32 v189, v189\n\t"
    "v_exp_f32 v190, v190\n\t"
    "v_exp_f32 v191, v191\n\t"
    "v_smfmac_f32_32x32x32_bf16 v[64:79], v[228:231], v[176:183], v236\n\t"
    "v_exp_f32 v192, v192\n\t"
    "s_waitcnt vmcnt(6) lgkmcnt(0)\n\t"
    "v_exp_f32 v193, v193\n\t"
    "s_add_u32 m0, %[ring], 6176\n\t"
    "v_exp_f32 v194, v194\n\t"
    "v_smfmac_f32_32x32x32_bf16 v[64:79], v[232:235], v[184:191], v236\n\t"
    "v_exp_f32 v195, v195\n\t"
    "s_add_u32 %[st], %[st], 40960\n\t"
    "v_exp_f32 v196, v196\n\t"
    "buffer_load_dwordx4 v237, %[srd], %[st] offen offset:0 sc1 lds\n\t"
    "v_exp_f32 v197, v197\n\t"
    "v_mfma_f32_32x32x16_bf16 v[176:191], v[222:225], v[152:155], 0\n\t"
    "v_exp_f32 v198, v198\n\t"
    "buffer_load_dwordx4 v238, %[srd], %[st] offen offset:1024 sc1 lds\n\t"
    "v_exp_f32 v199, v199\n\t"
    "buffer_load_dwordx4 v239, %[srd], %[st] offen offset:2048 sc1 lds\n\t"
    "v_exp_f32 v200, v200\n\t"
    "v_exp_f32 v201, v201\n\t"
    "v_mfma_f32_32x32x8bf16_1k v[176:191], v[226:227], v[172:173], v[176:191]\n\t"
    "v_exp_f32 v202, v202\n\t"
    "v_exp_f32 v203, v203\n\t"
    "v_exp_f32 v204, v204\n\t"
    "v_exp_f32 v205, v205\n\t"
    "v_exp_f32 v206, v206\n\t"
    "v_exp_f32 v207, v207\n\t"
    "v_smfmac_f32_32x32x32_bf16 v[80:95], v[228:231], v[192:199], v236\n\t"
    "v_exp_f32 v176, v176\n\t"
    "v_exp_f32 v177, v177\n\t"
    "v_exp_f32 v178, v178\n\t"
    "v_smfmac_f32_32x32x32_bf16 v[80:95], v[232:235], v[200:207], v236\n\t"
    "v_exp_f32 v179, v179\n\t"
    "v_exp_f32 v180, v180\n\t"
    "v_exp_f32 v181, v181\n\t"
    "v_mfma_f32_32x32x16_bf16 v[192:207], v[222:225], v[156:159], 0\n\t"
    "v_exp_f32 v182, v182\n\t"
    "v_exp_f32 v183, v183\n\t"
    "v_exp_f32 v184, v184\n\t"
    "v_exp_f32 v185, v185\n\t"
    "v_mfma_f32_32x32x8bf16_1k v[192:207], v[226:227], v[174:175], v[192:207]\n\t"
    "v_exp_f32 v186, v186\n\t"
    "v_exp_f32 v187, v187\n\t"
    "v_exp_f32 v188, v188\n\t"
    "v_exp_f32 v189, v189\n\t"
    "v_exp_f32 v190, v190\n\t"
    "v_exp_f32 v191, v191\n\t"
    "v_smfmac_f32_32x32x32_bf16 v[96:111], v[228:231], v[176:183], v236\n\t"
    "v_exp_f32 v192, v192\n\t"
    "v_exp_f32 v193, v193\n\t"
    "v_exp_f32 v194, v194\n\t"
    "v_smfmac_f32_32x32x32_bf16 v[96:111], v[232:235], v[184:191], v236\n\t"
    "v_exp_f32 v195, v195\n\t"
    "v_exp_f32 v196, v196\n\t"
    "v_exp_f32 v197, v197\n\t"
    "v_mfma_f32_32x32x16_bf16 v[176:191], v[208:211], v[128:131], 0\n\t"
    "v_exp_f32 v198, v198\n\t"
    "v_exp_f32 v199, v199\n\t"
    "v_exp_f32 v200, v200\n\t"
    "v_exp_f32 v201, v201\n\t"
    "v_mfma_f32_32x32x8bf16_1k v[176:191], v[212:213], v[160:161], v[176:191]\n\t"
    "v_exp_f32 v202, v202\n\t"
    "v_exp_f32 v203, v203\n\t"
    "v_exp_f32 v204, v204\n\t"
    "v_exp_f32 v205, v205\n\t"
    "v_exp_f32 v206, v206\n\t"
    "v_exp_f32 v207, v207\n\t"
    "v_smfmac_f32_32x32x32_bf16 v[112:127], v[228:231], v[192:199], v236\n\t"
    "v_exp_f32 v176, v176\n\t"
    "v_exp_f32 v177, v177\n\t"
    "v_exp_f32 v178, v178\n\t"
    "v_smfmac_f32_32x32x32_bf16 v[112:127], v[232:235], v[200:207], v236\n\t"
    "v_exp_f32 v179, v179\n\t"
    "v_exp_f32 v180, v180\n\t"
    "v_exp_f32 v181, v181\n\t"
    "v_mfma_f32_32x32x16_bf16 v[192:207], v[208:211], v[132:135], 0\n\t"
    "v_exp_f32 v182, v182\n\t"
    "v_exp_f32 v183, v183\n\t"
    "v_exp_f32 v184, v184\n\t"
    "v_exp_f32 v185, v185\n\t"
    "v_mfma_f32_32x32x8bf16_1k v[192:207], v[212:213], v[162:163], v[192:207]\n\t"
    "v_exp_f32 v186, v186\n\t"
    "v_exp_f32 v187, v187\n\t"
    "v_exp_f32 v188, v188\n\t"
    "v_exp_f32 v189, v189\n\t"
    "v_exp_f32 v190, v190\n\t"
    "v_exp_f32 v191, v191\n\t"
    "v_smfmac_f32_32x32x32_bf16 v[0:15], v[214:217], v[176:183], v236\n\t"
    "v_exp_f32 v192, v192\n\t"
    "ds_read_b128 v[222:225], v240 offset:9264\n\t"
    "v_exp_f32 v193, v193\n\t"
    "ds_read_b64 v[226:227], v241 offset:9264\n\t"
    "v_exp_f32 v194, v194\n\t"
    "v_smfmac_f32_32x32x32_bf16 v[0:15], v[218:221], v[184:191], v236\n\t"
    "v_exp_f32 v195, v195\n\t"
    "ds_read_b128 v[228:231], v242 offset:9264\n\t"
    "v_exp_f32 v196, v196\n\t"
    "ds_read_b128 v[232:235], v243 offset:9264\n\t"
    "v_exp_f32 v197, v197\n\t"
    "v_mfma_f32_32x32x16_bf16 v[176:191], v[208:211], v[136:139], 0\n\t"
    "v_exp_f32 v198, v198\n\t"
    "v_exp_f32 v199, v199\n\t"
    "v_exp_f32 v200, v200\n\t"
    "v_exp_f32 v201, v201\n\t"
    "v_mfma_f32_32x32x8bf16_1k v[176:191], v[212:213], v[164:165], v[176:191]\n\t"
    "v_exp_f32 v202, v202\n\t"
    "v_exp_f32 v203, v203\n\t"
    "v_exp_f32 v204, v204\n\t"
    "v_exp_f32 v205, v205\n\t"
    "v_exp_f32 v206, v206\n\t"
    "v_exp_f32 v207, v207\n\t"
    "v_smfmac_f32_32x32x32_bf16 v[16:31], v[214:217], v[192:199], v236\n\t"
    "v_exp_f32 v176, v176\n\t"
    "v_exp_f32 v177, v177\n\t"
    "v_exp_f32 v178, v178\n\t"
    "v_smfmac_f32_32x32x32_bf16 v[16:31], v[218:221], v[200:207], v236\n\t"
    "v_exp_f32 v179, v179\n\t"
    "v_exp_f32 v180, v180\n\t"
    "v_exp_f32 v181, v181\n\t"
    "v_mfma_f32_32x32x16_bf16 v[192:207], v[208:211], v[140:143], 0\n\t"
    "v_exp_f32 v182, v182\n\t"
    "v_exp_f32 v183, v183\n\t"
    "v_exp_f32 v184, v184\n\t"
    "v_exp_f32 v185, v185\n\t"
    "v_mfma_f32_32x32x8bf16_1k v[192:207], v[212:213], v[166:167], v[192:207]\n\t"
    "v_exp_f32 v186, v186\n\t"
    "v_exp_f32 v187, v187\n\t"
    "v_exp_f32 v188, v188\n\t"
    "v_exp_f32 v189, v189\n\t"
    "v_exp_f32 v190, v190\n\t"
    "v_exp_f32 v191, v191\n\t"
    "v_smfmac_f32_32x32x32_bf16 v[32:47], v[214:217], v[176:183], v236\n\t"
    "v_exp_f32 v192, v192\n\t"
    "v_exp_f32 v193, v193\n\t"
    "v_exp_f32 v194, v194\n\t"
    "v_smfmac_f32_32x32x32_bf16 v[32:47], v[218:221], v[184:191], v236\n\t"
    "v_exp_f32 v195, v195\n\t"
    "v_exp_f32 v196, v196\n\t"
    "v_exp_f32 v197, v197\n\t"
    "v_mfma_f32_32x32x16_bf16 v[176:191], v[208:211], v[144:147], 0\n\t"
    "v_exp_f32 v198, v198\n\t"
    "v_exp_f32 v199, v199\n\t"
    "v_exp_f32 v200, v200\n\t"
    "v_exp_f32 v201, v201\n\t"
    "v_mfma_f32_32x32x8bf16_1k v[176:191], v[212:213], v[168:169], v[176:191]\n\t"
    "v_exp_f32 v202, v202\n\t"
    "v_exp_f32 v203, v203\n\t"
    "v_exp_f32 v204, v204\n\t"
    "v_exp_f32 v205, v205\n\t"
    "v_exp_f32 v206, v206\n\t"
    "v_exp_f32 v207, v207\n\t"
    "v_smfmac_f32_32x32x32_bf16 v[48:63], v[214:217], v[192:199], v236\n\t"
    "v_exp_f32 v176, v176\n\t"
    "v_exp_f32 v177, v177\n\t"
    "v_exp_f32 v178, v178\n\t"
    "v_smfmac_f32_32x32x32_bf16 v[48:63], v[218:221], v[200:207], v236\n\t"
    "v_exp_f32 v179, v179\n\t"
    "v_exp_f32 v180, v180\n\t"
    "v_exp_f32 v181, v181\n\t"
    "v_mfma_f32_32x32x16_bf16 v[192:207], v[208:211], v[148:151], 0\n\t"
    "v_exp_f32 v182, v182\n\t"
    "v_exp_f32 v183, v183\n\t"
    "v_exp_f32 v184, v184\n\t"
    "v_exp_f32 v185, v185\n\t"
    "v_mfma_f32_32x32x8bf16_1k v[192:207], v[212:213], v[170:171], v[192:207]\n\t"
    "v_exp_f32 v186, v186\n\t"
    "v_exp_f32 v187, v187\n\t"
    "v_exp_f32 v188, v188\n\t"
    "v_exp_f32 v189, v189\n\t"
    "v_exp_f32 v190, v190\n\t"
    "v_exp_f32 v191, v191\n\t"
    "v_smfmac_f32_32x32x32_bf16 v[64:79], v[214:217], v[176:183], v236\n\t"
    "v_exp_f32 v192, v192\n\t"
    "s_waitcnt vmcnt(6) lgkmcnt(0)\n\t"
    "v_exp_f32 v193, v193\n\t"
    "s_add_u32 m0, %[ring], 9264\n\t"
    "v_exp_f32 v194, v194\n\t"
    "v_smfmac_f32_32x32x32_bf16 v[64:79], v[218:221], v[184:191], v236\n\t"
    "v_exp_f32 v195, v195\n\t"
    "s_add_u32 %[st], %[st], 40960\n\t"
    "v_exp_f32 v196, v196\n\t"
    "buffer_load_dwordx4 v237, %[srd], %[st] offen offset:0 sc1 lds\n\t"
    "v_exp_f32 v197, v197\n\t"
    "v_mfma_f32_32x32x16_bf16 v[176:191], v[208:211], v[152:155], 0\n\t"
    "v_exp_f32 v198, v198\n\t"
    "buffer_load_dwordx4 v238, %[srd], %[st] offen offset:1024 sc1 lds\n\t"
    "v_exp_f32 v199, v199\n\t"
    "buffer_load_dwordx4 v239, %[srd], %[st] offen offset:2048 sc1 lds\n\t"
    "v_exp_f32 v200, v200\n\t"
    "v_exp_f32 v201, v201\n\t"
    "v_mfma_f32_32x32x8bf16_1k v[176:191], v[212:213], v[172:173], v[176:191]\n\t"
    "v_exp_f32 v202, v202\n\t"
    "v_exp_f32 v203, v203\n\t"
    "v_exp_f32 v204, v204\n\t"
    "v_exp_f32 v205, v205\n\t"
    "v_exp_f32 v206, v206\n\t"
    "v_exp_f32 v207, v207\n\t"
    "v_smfmac_f32_32x32x32_bf16 v[80:95], v[214:217], v[192:199], v236\n\t"
    "v_exp_f32 v176, v176\n\t"
    "v_exp_f32 v177, v177\n\t"
    "v_exp_f32 v178, v178\n\t"
    "v_smfmac_f32_32x32x32_bf16 v[80:95], v[218:221], v[200:207], v236\n\t"
    "v_exp_f32 v179, v179\n\t"
    "v_exp_f32 v180, v180\n\t"
    "v_exp_f32 v181, v181\n\t"
    "v_mfma_f32_32x32x16_bf16 v[192:207], v[208:211], v[156:159], 0\n\t"
    "v_exp_f32 v182, v182\n\t"
    "v_exp_f32 v183, v183\n\t"
    "v_exp_f32 v184, v184\n\t"
    "v_exp_f32 v185, v185\n\t"
    "v_mfma_f32_32x32x8bf16_1k v[192:207], v[212:213], v[174:175], v[192:207]\n\t"
    "v_exp_f32 v186, v186\n\t"
    "v_exp_f32 v187, v187\n\t"
    "v_exp_f32 v188, v188\n\t"
    "v_exp_f32 v189, v189\n\t"
    "v_exp_f32 v190, v190\n\t"
    "v_exp_f32 v191, v191\n\t"
    "v_smfmac_f32_32x32x32_bf16 v[96:111], v[214:217], v[176:183], v236\n\t"
    "v_exp_f32 v192, v192\n\t"
    "v_exp_f32 v193, v193\n\t"
    "v_exp_f32 v194, v194\n\t"
    "v_smfmac_f32_32x32x32_bf16 v[96:111], v[218:221], v[184:191], v236\n\t"
    "v_exp_f32 v195, v195\n\t"
    "v_exp_f32 v196, v196\n\t"
    "v_exp_f32 v197, v197\n\t"
    "v_mfma_f32_32x32x16_bf16 v[176:191], v[222:225], v[128:131], 0\n\t"
    "v_exp_f32 v198, v198\n\t"
    "v_exp_f32 v199, v199\n\t"
    "v_exp_f32 v200, v200\n\t"
    "v_exp_f32 v201, v201\n\t"
    "v_mfma_f32_32x32x8bf16_1k v[176:191], v[226:227], v[160:161], v[176:191]\n\t"
    "v_exp_f32 v202, v202\n\t"
    "v_exp_f32 v203, v203\n\t"
    "v_exp_f32 v204, v204\n\t"
    "v_exp_f32 v205, v205\n\t"
    "v_exp_f32 v206, v206\n\t"
    "v_exp_f32 v207, v207\n\t"
    "v_smfmac_f32_32x32x32_bf16 v[112:127], v[214:217], v[192:199], v236\n\t"
    "v_exp_f32 v176, v176\n\t"
    "v_exp_f32 v177, v177\n\t"
    "v_exp_f32 v178, v178\n\t"
    "v_smfmac_f32_32x32x32_bf16 v[112:127], v[218:221], v[200:207], v236\n\t"
    "v_exp_f32 v179, v179\n\t"
    "v_exp_f32 v180, v180\n\t"
    "v_exp_f32 v181, v181\n\t"
    "v_mfma_f32_32x32x16_bf16 v[192:207], v[222:225], v[132:135], 0\n\t"
    "v_exp_f32 v182, v182\n\t"
    "v_exp_f32 v183, v183\n\t"
    "v_exp_f32 v184, v184\n\t"
    "v_exp_f32 v185, v185\n\t"
    "v_mfma_f32_32x32x8bf16_1k v[192:207], v[226:227], v[162:163], v[192:207]\n\t"
    "v_exp_f32 v186, v186\n\t"
    "v_exp_f32 v187, v187\n\t"
    "v_exp_f32 v188, v188\n\t"
    "v_exp_f32 v189, v189\n\t"
    "v_exp_f32 v190, v190\n\t"
    "v_exp_f32 v191, v191\n\t"
    "v_smfmac_f32_32x32x32_bf16 v[0:15], v[228:231], v[176:183], v236\n\t"
    "v_exp_f32 v192, v192\n\t"
    "ds_read_b128 v[208:211], v240 offset:0\n\t"
    "v_exp_f32 v193, v193\n\t"
    "ds_read_b64 v[212:213], v241 offset:0\n\t"
    "v_exp_f32 v194, v194\n\t"
    "v_smfmac_f32_32x32x32_bf16 v[0:15], v[232:235], v[184:191], v236\n\t"
    "v_exp_f32 v195, v195\n\t"
    "ds_read_b128 v[214:217], v242 offset:0\n\t"
    "v_exp_f32 v196, v196\n\t"
    "ds_read_b128 v[218:221], v243 offset:0\n\t"
    "v_exp_f32 v197, v197\n\t"
    "v_mfma_f32_32x32x16_bf16 v[176:191], v[222:225], v[136:139], 0\n\t"
    "v_exp_f32 v198, v198\n\t"
    "v_exp_f32 v199, v199\n\t"
    "v_exp_f32 v200, v200\n\t"
    "v_exp_f32 v201, v201\n\t"
    "v_mfma_f32_32x32x8bf16_1k v[176:191], v[226:227], v[164:165], v[176:191]\n\t"
    "v_exp_f32 v202, v202\n\t"
    "v_exp_f32 v203, v203\n\t"
    "v_exp_f32 v204, v204\n\t"
    "v_exp_f32 v205, v205\n\t"
    "v_exp_f32 v206, v206\n\t"
    "v_exp_f32 v207, v207\n\t"
    "v_smfmac_f32_32x32x32_bf16 v[16:31], v[228:231], v[192:199], v236\n\t"
    "v_exp_f32 v176, v176\n\t"
    "v_exp_f32 v177, v177\n\t"
    "v_exp_f32 v178, v178\n\t"
    "v_smfmac_f32_32x32x32_bf16 v[16:31], v[232:235], v[200:207], v236\n\t"
    "v_exp_f32 v179, v179\n\t"
    "v_exp_f32 v180, v180\n\t"
    "v_exp_f32 v181, v181\n\t"
    "v_mfma_f32_32x32x16_bf16 v[192:207], v[222:225], v[140:143], 0\n\t"
    "v_exp_f32 v182, v182\n\t"
    "v_exp_f32 v183, v183\n\t"
    "v_exp_f32 v184, v184\n\t"
    "v_exp_f32 v185, v185\n\t"
    "v_mfma_f32_32x32x8bf16_1k v[192:207], v[226:227], v[166:167], v[192:207]\n\t"
    "v_exp_f32 v186, v186\n\t"
    "v_exp_f32 v187, v187\n\t"
    "v_exp_f32 v188, v188\n\t"
    "v_exp_f32 v189, v189\n\t"
    "v_exp_f32 v190, v190\n\t"
    "v_exp_f32 v191, v191\n\t"
    "v_smfmac_f32_32x32x32_bf16 v[32:47], v[228:231], v[176:183], v236\n\t"
    "v_exp_f32 v192, v192\n\t"
    "v_exp_f32 v193, v193\n\t"
    "v_exp_f32 v194, v194\n\t"
    "v_smfmac_f32_32x32x32_bf16 v[32:47], v[232:235], v[184:191], v236\n\t"
    "v_exp_f32 v195, v195\n\t"
    "v_exp_f32 v196, v196\n\t"
    "v_exp_f32 v197, v197\n\t"
    "v_mfma_f32_32x32x16_bf16 v[176:191], v[222:225], v[144:147], 0\n\t"
    "v_exp_f32 v198, v198\n\t"
    "v_exp_f32 v199, v199\n\t"
    "v_exp_f32 v200, v200\n\t"
    "v_exp_f32 v201, v201\n\t"
    "v_mfma_f32_32x32x8bf16_1k v[176:191], v[226:227], v[168:169], v[176:191]\n\t"
    "v_exp_f32 v202, v202\n\t"
    "v_exp_f32 v203, v203\n\t"
    "v_exp_f32 v204, v204\n\t"
    "v_exp_f32 v205, v205\n\t"
    "v_exp_f32 v206, v206\n\t"
    "v_exp_f32 v207, v207\n\t"
    "v_smfmac_f32_32x32x32_bf16 v[48:63], v[228:231], v[192:199], v236\n\t"
    "v_exp_f32 v176, v176\n\t"
    "v_exp_f32 v177, v177\n\t"
    "v_exp_f32 v178, v178\n\t"
    "v_smfmac_f32_32x32x32_bf16 v[48:63], v[232:235], v[200:207], v236\n\t"
    "v_exp_f32 v179, v179\n\t"
    "v_exp_f32 v180, v180\n\t"
    "v_exp_f32 v181, v181\n\t"
    "v_mfma_f32_32x32x16_bf16 v[192:207], v[222:225], v[148:151], 0\n\t"
    "v_exp_f32 v182, v182\n\t"
    "v_exp_f32 v183, v183\n\t"
    "v_exp_f32 v184, v184\n\t"
    "v_exp_f32 v185, v185\n\t"
    "v_mfma_f32_32x32x8bf16_1k v[192:207], v[226:227], v[170:171], v[192:207]\n\t"
    "v_exp_f32 v186, v186\n\t"
    "v_exp_f32 v187, v187\n\t"
    "v_exp_f32 v188, v188\n\t"
    "v_exp_f32 v189, v189\n\t"
    "v_exp_f32 v190, v190\n\t"
    "v_exp_f32 v191, v191\n\t"
    "v_smfmac_f32_32x32x32_bf16 v[64:79], v[228:231], v[176:183], v236\n\t"
    "v_exp_f32 v192, v192\n\t"
    "s_waitcnt vmcnt(6) lgkmcnt(0)\n\t"
    "v_exp_f32 v193, v193\n\t"
    "s_add_u32 m0, %[ring], 0\n\t"
    "v_exp_f32 v194, v194\n\t"
    "v_smfmac_f32_32x32x32_bf16 v[64:79], v[232:235], v[184:191], v236\n\t"
    "v_exp_f32 v195, v195\n\t"
    "s_add_u32 %[st], %[st], 40960\n\t"
    "v_exp_f32 v196, v196\n\t"
    "buffer_load_dwordx4 v237, %[srd], %[st] offen offset:0 sc1 lds\n\t"
    "v_exp_f32 v197, v197\n\t"
    "v_mfma_f32_32x32x16_bf16 v[176:191], v[222:225], v[152:155], 0\n\t"
    "v_exp_f32 v198, v198\n\t"
    "buffer_load_dwordx4 v238, %[srd], %[st] offen offset:1024 sc1 lds\n\t"
    "v_exp_f32 v199, v199\n\t"
    "buffer_load_dwordx4 v239, %[srd], %[st] offen offset:2048 sc1 lds\n\t"
    "v_exp_f32 v200, v200\n\t"
    "v_exp_f32 v201, v201\n\t"
    "v_mfma_f32_32x32x8bf16_1k v[176:191], v[226:227], v[172:173], v[176:191]\n\t"
    "v_exp_f32 v202, v202\n\t"
    "v_exp_f32 v203, v203\n\t"
    "v_exp_f32 v204, v204\n\t"
    "v_exp_f32 v205, v205\n\t"
    "v_exp_f32 v206, v206\n\t"
    "v_exp_f32 v207, v207\n\t"
    "v_smfmac_f32_32x32x32_bf16 v[80:95], v[228:231], v[192:199], v236\n\t"
    "v_exp_f32 v176, v176\n\t"
    "v_exp_f32 v177, v177\n\t"
    "v_exp_f32 v178, v178\n\t"
    "v_smfmac_f32_32x32x32_bf16 v[80:95], v[232:235], v[200:207], v236\n\t"
    "v_exp_f32 v179, v179\n\t"
    "v_exp_f32 v180, v180\n\t"
    "v_exp_f32 v181, v181\n\t"
    "v_mfma_f32_32x32x16_bf16 v[192:207], v[222:225], v[156:159], 0\n\t"
    "v_exp_f32 v182, v182\n\t"
    "v_exp_f32 v183, v183\n\t"
    "v_exp_f32 v184, v184\n\t"
    "v_exp_f32 v185, v185\n\t"
    "v_mfma_f32_32x32x8bf16_1k v[192:207], v[226:227], v[174:175], v[192:207]\n\t"
    "v_exp_f32 v186, v186\n\t"
    "v_exp_f32 v187, v187\n\t"
    "v_exp_f32 v188, v188\n\t"
    "v_exp_f32 v189, v189\n\t"
    "v_exp_f32 v190, v190\n\t"
    "v_exp_f32 v191, v191\n\t"
    "v_smfmac_f32_32x32x32_bf16 v[96:111], v[228:231], v[176:183], v236\n\t"
    "v_exp_f32 v192, v192\n\t"
    "v_exp_f32 v193, v193\n\t"
    "v_exp_f32 v194, v194\n\t"
    "v_smfmac_f32_32x32x32_bf16 v[96:111], v[232:235], v[184:191], v236\n\t"
    "v_exp_f32 v195, v195\n\t"
    "v_exp_f32 v196, v196\n\t"
    "v_exp_f32 v197, v197\n\t"
    "v_mfma_f32_32x32x16_bf16 v[176:191], v[208:211], v[128:131], 0\n\t"
    "v_exp_f32 v198, v198\n\t"
    "v_exp_f32 v199, v199\n\t"
    "v_exp_f32 v200, v200\n\t"
    "v_exp_f32 v201, v201\n\t"
    "v_mfma_f32_32x32x8bf16_1k v[176:191], v[212:213], v[160:161], v[176:191]\n\t"
    "v_exp_f32 v202, v202\n\t"
    "v_exp_f32 v203, v203\n\t"
    "v_exp_f32 v204, v204\n\t"
    "v_exp_f32 v205, v205\n\t"
    "v_exp_f32 v206, v206\n\t"
    "v_exp_f32 v207, v207\n\t"
    "s_sub_u32 %[t], %[t], 1\n\t"
    "s_cmp_lg_u32 %[t], 0\n\t"
    "s_cbranch_scc1 .LLA_%=\n\t"
    "v_smfmac_f32_32x32x32_bf16 v[112:127], v[228:231], v[192:199], v236\n\t"
    "v_exp_f32 v176, v176\n\t"
    "v_exp_f32 v177, v177\n\t"
    "v_exp_f32 v178, v178\n\t"
    "v_smfmac_f32_32x32x32_bf16 v[112:127], v[232:235], v[200:207], v236\n\t"
    "v_exp_f32 v179, v179\n\t"
    "v_exp_f32 v180, v180\n\t"
    "v_exp_f32 v181, v181\n\t"
    "v_mfma_f32_32x32x16_bf16 v[192:207], v[208:211], v[132:135], 0\n\t"
    "v_exp_f32 v182, v182\n\t"
    "v_exp_f32 v183, v183\n\t"
    "v_exp_f32 v184, v184\n\t"
    "v_exp_f32 v185, v185\n\t"
    "v_mfma_f32_32x32x8bf16_1k v[192:207], v[212:213], v[162:163], v[192:207]\n\t"
    "v_exp_f32 v186, v186\n\t"
    "v_exp_f32 v187, v187\n\t"
    "v_exp_f32 v188, v188\n\t"
    "v_exp_f32 v189, v189\n\t"
    "v_exp_f32 v190, v190\n\t"
    "v_exp_f32 v191, v191\n\t"
    "v_smfmac_f32_32x32x32_bf16 v[0:15], v[214:217], v[176:183], v236\n\t"
    "v_exp_f32 v192, v192\n\t"
    "ds_read_b128 v[222:225], v240 offset:3088\n\t"
    "v_exp_f32 v193, v193\n\t"
    "ds_read_b64 v[226:227], v241 offset:3088\n\t"
    "v_exp_f32 v194, v194\n\t"
    "v_smfmac_f32_32x32x32_bf16 v[0:15], v[218:221], v[184:191], v236\n\t"
    "v_exp_f32 v195, v195\n\t"
    "ds_read_b128 v[228:231], v242 offset:3088\n\t"
    "v_exp_f32 v196, v196\n\t"
    "ds_read_b128 v[232:235], v243 offset:3088\n\t"
    "v_exp_f32 v197, v197\n\t"
    "v_mfma_f32_32x32x16_bf16 v[176:191], v[208:211], v[136:139], 0\n\t"
    "v_exp_f32 v198, v198\n\t"
    "v_exp_f32 v199, v199\n\t"
    "v_exp_f32 v200, v200\n\t"
    "v_exp_f32 v201, v201\n\t"
    "v_mfma_f32_32x32x8bf16_1k v[176:191], v[212:213], v[164:165], v[176:191]\n\t"
    "v_exp_f32 v202, v202\n\t"
    "v_exp_f32 v203, v203\n\t"
    "v_exp_f32 v204, v204\n\t"
    "v_exp_f32 v205, v205\n\t"
    "v_exp_f32 v206, v206\n\t"
    "v_exp_f32 v207, v207\n\t"
    "v_smfmac_f32_32x32x32_bf16 v[16:31], v[214:217], v[192:199], v236\n\t"
    "v_exp_f32 v176, v176\n\t"
    "v_exp_f32 v177, v177\n\t"
    "v_exp_f32 v178, v178\n\t"
    "v_smfmac_f32_32x32x32_bf16 v[16:31], v[218:221], v[200:207], v236\n\t"
    "v_exp_f32 v179, v179\n\t"
    "v_exp_f32 v180, v180\n\t"
    "v_exp_f32 v181, v181\n\t"
    "v_mfma_f32_32x32x16_bf16 v[192:207], v[208:211], v[140:143], 0\n\t"
    "v_exp_f32 v182, v182\n\t"
    "v_exp_f32 v183, v183\n\t"
    "v_exp_f32 v184, v184\n\t"
    "v_exp_f32 v185, v185\n\t"
    "v_mfma_f32_32x32x8bf16_1k v[192:207], v[212:213], v[166:167], v[192:207]\n\t"
    "v_exp_f32 v186, v186\n\t"
    "v_exp_f32 v187, v187\n\t"
    "v_exp_f32 v188, v188\n\t"
    "v_exp_f32 v189, v189\n\t"
    "v_exp_f32 v190, v190\n\t"
    "v_exp_f32 v191, v191\n\t"
    "v_smfmac_f32_32x32x32_bf16 v[32:47], v[214:217], v[176:183], v236\n\t"
    "v_exp_f32 v192, v192\n\t"
    "v_exp_f32 v193, v193\n\t"
    "v_exp_f32 v194, v194\n\t"
    "v_smfmac_f32_32x32x32_bf16 v[32:47], v[218:221], v[184:191], v236\n\t"
    "v_exp_f32 v195, v195\n\t"
    "v_exp_f32 v196, v196\n\t"
    "v_exp_f32 v197, v197\n\t"
    "v_mfma_f32_32x32x16_bf16 v[176:191], v[208:211], v[144:147], 0\n\t"
    "v_exp_f32 v198, v198\n\t"
    "v_exp_f32 v199, v199\n\t"
    "v_exp_f32 v200, v200\n\t"
    "v_exp_f32 v201, v201\n\t"
    "v_mfma_f32_32x32x8bf16_1k v[176:191], v[212:213], v[168:169], v[176:191]\n\t"
    "v_exp_f32 v202, v202\n\t"
    "v_exp_f32 v203, v203\n\t"
    "v_exp_f32 v204, v204\n\t"
    "v_exp_f32 v205, v205\n\t"
    "v_exp_f32 v206, v206\n\t"
    "v_exp_f32 v207, v207\n\t"
    "v_smfmac_f32_32x32x32_bf16 v[48:63], v[214:217], v[192:199], v236\n\t"
    "v_exp_f32 v176, v176\n\t"
    "v_exp_f32 v177, v177\n\t"
    "v_exp_f32 v178, v178\n\t"
    "v_smfmac_f32_32x32x32_bf16 v[48:63], v[218:221], v[200:207], v236\n\t"
    "v_exp_f32 v179, v179\n\t"
    "v_exp_f32 v180, v180\n\t"
    "v_exp_f32 v181, v181\n\t"
    "v_mfma_f32_32x32x16_bf16 v[192:207], v[208:211], v[148:151], 0\n\t"
    "v_exp_f32 v182, v182\n\t"
    "v_exp_f32 v183, v183\n\t"
    "v_exp_f32 v184, v184\n\t"
    "v_exp_f32 v185, v185\n\t"
    "v_mfma_f32_32x32x8bf16_1k v[192:207], v[212:213], v[170:171], v[192:207]\n\t"
    "v_exp_f32 v186, v186\n\t"
    "v_exp_f32 v187, v187\n\t"
    "v_exp_f32 v188, v188\n\t"
    "v_exp_f32 v189, v189\n\t"
    "v_exp_f32 v190, v190\n\t"
    "v_exp_f32 v191, v191\n\t"
    "v_smfmac_f32_32x32x32_bf16 v[64:79], v[214:217], v[176:183], v236\n\t"
    "v_exp_f32 v192, v192\n\t"
    "s_waitcnt vmcnt(6) lgkmcnt(0)\n\t"
    "v_exp_f32 v193, v193\n\t"
    "v_exp_f32 v194, v194\n\t"
    "v_smfmac_f32_32x32x32_bf16 v[64:79], v[218:221], v[184:191], v236\n\t"
    "v_exp_f32 v195, v195\n\t"
    "v_exp_f32 v196, v196\n\t"
    "v_exp_f32 v197, v197\n\t"
    "v_mfma_f32_32x32x16_bf16 v[176:191], v[208:211], v[152:155], 0\n\t"
    "v_exp_f32 v198, v198\n\t"
    "v_exp_f32 v199, v199\n\t"
    "v_exp_f32 v200, v200\n\t"
    "v_exp_f32 v201, v201\n\t"
    "v_mfma_f32_32x32x8bf16_1k v[176:191], v[212:213], v[172:173], v[176:191]\n\t"
    "v_exp_f32 v202, v202\n\t"
    "v_exp_f32 v203, v203\n\t"
    "v_exp_f32 v204, v204\n\t"
    "v_exp_f32 v205, v205\n\t"
    "v_exp_f32 v206, v206\n\t"
    "v_exp_f32 v207, v207\n\t"
    "v_smfmac_f32_32x32x32_bf16 v[80:95], v[214:217], v[192:199], v236\n\t"
    "v_exp_f32 v176, v176\n\t"
    "v_exp_f32 v177, v177\n\t"
    "v_exp_f32 v178, v178\n\t"
    "v_smfmac_f32_32x32x32_bf16 v[80:95], v[218:221], v[200:207], v236\n\t"
    "v_exp_f32 v179, v179\n\t"
    "v_exp_f32 v180, v180\n\t"
    "v_exp_f32 v181, v181\n\t"
    "v_mfma_f32_32x32x16_bf16 v[192:207], v[208:211], v[156:159], 0\n\t"
    "v_exp_f32 v182, v182\n\t"
    "v_exp_f32 v183, v183\n\t"
    "v_exp_f32 v184, v184\n\t"
    "v_exp_f32 v185, v185\n\t"
    "v_mfma_f32_32x32x8bf16_1k v[192:207], v[212:213], v[174:175], v[192:207]\n\t"
    "v_exp_f32 v186, v186\n\t"
    "v_exp_f32 v187, v187\n\t"
    "v_exp_f32 v188, v188\n\t"
    "v_exp_f32 v189, v189\n\t"
    "v_exp_f32 v190, v190\n\t"
    "v_exp_f32 v191, v191\n\t"
    "v_smfmac_f32_32x32x32_bf16 v[96:111], v[214:217], v[176:183], v236\n\t"
    "v_exp_f32 v192, v192\n\t"
    "v_exp_f32 v193, v193\n\t"
    "v_exp_f32 v194, v194\n\t"
    "v_smfmac_f32_32x32x32_bf16 v[96:111], v[218:221], v[184:191], v236\n\t"
    "v_exp_f32 v195, v195\n\t"
    "v_exp_f32 v196, v196\n\t"
    "v_exp_f32 v197, v197\n\t"
    "v_mfma_f32_32x32x16_bf16 v[176:191], v[222:225], v[128:131], 0\n\t"
    "v_exp_f32 v198, v198\n\t"
    "v_exp_f32 v199, v199\n\t"
    "v_exp_f32 v200, v200\n\t"
    "v_exp_f32 v201, v201\n\t"
    "v_mfma_f32_32x32x8bf16_1k v[176:191], v[226:227], v[160:161], v[176:191]\n\t"
    "v_exp_f32 v202, v202\n\t"
    "v_exp_f32 v203, v203\n\t"
    "v_exp_f32 v204, v204\n\t"
    "v_exp_f32 v205, v205\n\t"
    "v_exp_f32 v206, v206\n\t"
    "v_exp_f32 v207, v207\n\t"
    "v_smfmac_f32_32x32x32_bf16 v[112:127], v[214:217], v[192:199], v236\n\t"
    "v_exp_f32 v176, v176\n\t"
    "v_exp_f32 v177, v177\n\t"
    "v_exp_f32 v178, v178\n\t"
    "v_smfmac_f32_32x32x32_bf16 v[112:127], v[218:221], v[200:207], v236\n\t"
    "v_exp_f32 v179, v179\n\t"
    "v_exp_f32 v180, v180\n\t"
    "v_exp_f32 v181, v181\n\t"
    "v_mfma_f32_32x32x16_bf16 v[192:207], v[222:225], v[132:135], 0\n\t"
    "v_exp_f32 v182, v182\n\t"
    "v_exp_f32 v183, v183\n\t"
    "v_exp_f32 v184, v184\n\t"
    "v_exp_f32 v185, v185\n\t"
    "v_mfma_f32_32x32x8bf16_1k v[192:207], v[226:227], v[162:163], v[192:207]\n\t"
    "v_exp_f32 v186, v186\n\t"
    "v_exp_f32 v187, v187\n\t"
    "v_exp_f32 v188, v188\n\t"
    "v_exp_f32 v189, v189\n\t"
    "v_exp_f32 v190, v190\n\t"
    "v_exp_f32 v191, v191\n\t"
    "v_smfmac_f32_32x32x32_bf16 v[0:15], v[228:231], v[176:183], v236\n\t"
    "v_exp_f32 v192, v192\n\t"
    "ds_read_b128 v[208:211], v240 offset:6176\n\t"
    "v_exp_f32 v193, v193\n\t"
    "ds_read_b64 v[212:213], v241 offset:6176\n\t"
    "v_exp_f32 v194, v194\n\t"
    "v_smfmac_f32_32x32x32_bf16 v[0:15], v[232:235], v[184:191], v236\n\t"
    "v_exp_f32 v195, v195\n\t"
    "ds_read_b128 v[214:217], v242 offset:6176\n\t"
    "v_exp_f32 v196, v196\n\t"
    "ds_read_b128 v[218:221], v243 offset:6176\n\t"
    "v_exp_f32 v197, v197\n\t"
    "v_mfma_f32_32x32x16_bf16 v[176:191], v[222:225], v[136:139], 0\n\t"
    "v_exp_f32 v198, v198\n\t"
    "v_exp_f32 v199, v199\n\t"
    "v_exp_f32 v200, v200\n\t"
    "v_exp_f32 v201, v201\n\t"
    "v_mfma_f32_32x32x8bf16_1k v[176:191], v[226:227], v[164:165], v[176:191]\n\t"
    "v_exp_f32 v202, v202\n\t"
    "v_exp_f32 v203, v203\n\t"
    "v_exp_f32 v204, v204\n\t"
    "v_exp_f32 v205, v205\n\t"
    "v_exp_f32 v206, v206\n\t"
    "v_exp_f32 v207, v207\n\t"
    "v_smfmac_f32_32x32x32_bf16 v[16:31], v[228:231], v[192:199], v236\n\t"
    "v_exp_f32 v176, v176\n\t"
    "v_exp_f32 v177, v177\n\t"
    "v_exp_f32 v178, v178\n\t"
    "v_smfmac_f32_32x32x32_bf16 v[16:31], v[232:235], v[200:207], v236\n\t"
    "v_exp_f32 v179, v179\n\t"
    "v_exp_f32 v180, v180\n\t"
    "v_exp_f32 v181, v181\n\t"
    "v_mfma_f32_32x32x16_bf16 v[192:207], v[222:225], v[140:143], 0\n\t"
    "v_exp_f32 v182, v182\n\t"
    "v_exp_f32 v183, v183\n\t"
    "v_exp_f32 v184, v184\n\t"
    "v_exp_f32 v185, v185\n\t"
    "v_mfma_f32_32x32x8bf16_1k v[192:207], v[226:227], v[166:167], v[192:207]\n\t"
    "v_exp_f32 v186, v186\n\t"
    "v_exp_f32 v187, v187\n\t"
    "v_exp_f32 v188, v188\n\t"
    "v_exp_f32 v189, v189\n\t"
    "v_exp_f32 v190, v190\n\t"
    "v_exp_f32 v191, v191\n\t"
    "v_smfmac_f32_32x32x32_bf16 v[32:47], v[228:231], v[176:183], v236\n\t"
    "v_exp_f32 v192, v192\n\t"
    "v_exp_f32 v193, v193\n\t"
    "v_exp_f32 v194, v194\n\t"
    "v_smfmac_f32_32x32x32_bf16 v[32:47], v[232:235], v[184:191], v236\n\t"
    "v_exp_f32 v195, v195\n\t"
    "v_exp_f32 v196, v196\n\t"
    "v_exp_f32 v197, v197\n\t"
    "v_mfma_f32_32x32x16_bf16 v[176:191], v[222:225], v[144:147], 0\n\t"
    "v_exp_f32 v198, v198\n\t"
    "v_exp_f32 v199, v199\n\t"
    "v_exp_f32 v200, v200\n\t"
    "v_exp_f32 v201, v201\n\t"
    "v_mfma_f32_32x32x8bf16_1k v[176:191], v[226:227], v[168:169], v[176:191]\n\t"
    "v_exp_f32 v202, v202\n\t"
    "v_exp_f32 v203, v203\n\t"
    "v_exp_f32 v204, v204\n\t"
    "v_exp_f32 v205, v205\n\t"
    "v_exp_f32 v206, v206\n\t"
    "v_exp_f32 v207, v207\n\t"
    "v_smfmac_f32_32x32x32_bf16 v[48:63], v[228:231], v[192:199], v236\n\t"
    "v_exp_f32 v176, v176\n\t"
    "v_exp_f32 v177, v177\n\t"
    "v_exp_f32 v178, v178\n\t"
    "v_smfmac_f32_32x32x32_bf16 v[48:63], v[232:235], v[200:207], v236\n\t"
    "v_exp_f32 v179, v179\n\t"
    "v_exp_f32 v180, v180\n\t"
    "v_exp_f32 v181, v181\n\t"
    "v_mfma_f32_32x32x16_bf16 v[192:207], v[222:225], v[148:151], 0\n\t"
    "v_exp_f32 v182, v182\n\t"
    "v_exp_f32 v183, v183\n\t"
    "v_exp_f32 v184, v184\n\t"
    "v_exp_f32 v185, v185\n\t"
    "v_mfma_f32_32x32x8bf16_1k v[192:207], v[226:227], v[170:171], v[192:207]\n\t"
    "v_exp_f32 v186, v186\n\t"
    "v_exp_f32 v187, v187\n\t"
    "v_exp_f32 v188, v188\n\t"
    "v_exp_f32 v189, v189\n\t"
    "v_exp_f32 v190, v190\n\t"
    "v_exp_f32 v191, v191\n\t"
    "v_smfmac_f32_32x32x32_bf16 v[64:79], v[228:231], v[176:183], v236\n\t"
    "v_exp_f32 v192, v192\n\t"
    "s_waitcnt vmcnt(3) lgkmcnt(0)\n\t"
    "v_exp_f32 v193, v193\n\t"
    "v_exp_f32 v194, v194\n\t"
    "v_smfmac_f32_32x32x32_bf16 v[64:79], v[232:235], v[184:191], v236\n\t"
    "v_exp_f32 v195, v195\n\t"
    "v_exp_f32 v196, v196\n\t"
    "v_exp_f32 v197, v197\n\t"
    "v_mfma_f32_32x32x16_bf16 v[176:191], v[222:225], v[152:155], 0\n\t"
    "v_exp_f32 v198, v198\n\t"
    "v_exp_f32 v199, v199\n\t"
    "v_exp_f32 v200, v200\n\t"
    "v_exp_f32 v201, v201\n\t"
    "v_mfma_f32_32x32x8bf16_1k v[176:191], v[226:227], v[172:173], v[176:191]\n\t"
    "v_exp_f32 v202, v202\n\t"
    "v_exp_f32 v203, v203\n\t"
    "v_exp_f32 v204, v204\n\t"
    "v_exp_f32 v205, v205\n\t"
    "v_exp_f32 v206, v206\n\t"
    "v_exp_f32 v207, v207\n\t"
    "v_smfmac_f32_32x32x32_bf16 v[80:95], v[228:231], v[192:199], v236\n\t"
    "v_exp_f32 v176, v176\n\t"
    "v_exp_f32 v177, v177\n\t"
    "v_exp_f32 v178, v178\n\t"
    "v_smfmac_f32_32x32x32_bf16 v[80:95], v[232:235], v[200:207], v236\n\t"
    "v_exp_f32 v179, v179\n\t"
    "v_exp_f32 v180, v180\n\t"
    "v_exp_f32 v181, v181\n\t"
    "v_mfma_f32_32x32x16_bf16 v[192:207], v[222:225], v[156:159], 0\n\t"
    "v_exp_f32 v182, v182\n\t"
    "v_exp_f32 v183, v183\n\t"
    "v_exp_f32 v184, v184\n\t"
    "v_exp_f32 v185, v185\n\t"
    "v_mfma_f32_32x32x8bf16_1k v[192:207], v[226:227], v[174:175], v[192:207]\n\t"
    "v_exp_f32 v186, v186\n\t"
    "v_exp_f32 v187, v187\n\t"
    "v_exp_f32 v188, v188\n\t"
    "v_exp_f32 v189, v189\n\t"
    "v_exp_f32 v190, v190\n\t"
    "v_exp_f32 v191, v191\n\t"
    "v_smfmac_f32_32x32x32_bf16 v[96:111], v[228:231], v[176:183], v236\n\t"
    "v_exp_f32 v192, v192\n\t"
    "v_exp_f32 v193, v193\n\t"
    "v_exp_f32 v194, v194\n\t"
    "v_smfmac_f32_32x32x32_bf16 v[96:111], v[232:235], v[184:191], v236\n\t"
    "v_exp_f32 v195, v195\n\t"
    "v_exp_f32 v196, v196\n\t"
    "v_exp_f32 v197, v197\n\t"
    "v_mfma_f32_32x32x16_bf16 v[176:191], v[208:211], v[128:131], 0\n\t"
    "v_exp_f32 v198, v198\n\t"
    "v_exp_f32 v199, v199\n\t"
    "v_exp_f32 v200, v200\n\t"
    "v_exp_f32 v201, v201\n\t"
    "v_mfma_f32_32x32x8bf16_1k v[176:191], v[212:213], v[160:161], v[176:191]\n\t"
    "v_exp_f32 v202, v202\n\t"
    "v_exp_f32 v203, v203\n\t"
    "v_exp_f32 v204, v204\n\t"
    "v_exp_f32 v205, v205\n\t"
    "v_exp_f32 v206, v206\n\t"
    "v_exp_f32 v207, v207\n\t"
    "v_smfmac_f32_32x32x32_bf16 v[112:127], v[228:231], v[192:199], v236\n\t"
    "v_exp_f32 v176, v176\n\t"
    "v_exp_f32 v177, v177\n\t"
    "v_exp_f32 v178, v178\n\t"
    "v_smfmac_f32_32x32x32_bf16 v[112:127], v[232:235], v[200:207], v236\n\t"
    "v_exp_f32 v179, v179\n\t"
    "v_exp_f32 v180, v180\n\t"
    "v_exp_f32 v181, v181\n\t"
    "v_mfma_f32_32x32x16_bf16 v[192:207], v[208:211], v[132:135], 0\n\t"
    "v_exp_f32 v182, v182\n\t"
    "v_exp_f32 v183, v183\n\t"
    "v_exp_f32 v184, v184\n\t"
    "v_exp_f32 v185, v185\n\t"
    "v_mfma_f32_32x32x8bf16_1k v[192:207], v[212:213], v[162:163], v[192:207]\n\t"
    "v_exp_f32 v186, v186\n\t"
    "v_exp_f32 v187, v187\n\t"
    "v_exp_f32 v188, v188\n\t"
    "v_exp_f32 v189, v189\n\t"
    "v_exp_f32 v190, v190\n\t"
    "v_exp_f32 v191, v191\n\t"
    "v_smfmac_f32_32x32x32_bf16 v[0:15], v[214:217], v[176:183], v236\n\t"
    "v_exp_f32 v192, v192\n\t"
    "ds_read_b128 v[222:225], v240 offset:9264\n\t"
    "v_exp_f32 v193, v193\n\t"
    "ds_read_b64 v[226:227], v241 offset:9264\n\t"
    "v_exp_f32 v194, v194\n\t"
    "v_smfmac_f32_32x32x32_bf16 v[0:15], v[218:221], v[184:191], v236\n\t"
    "v_exp_f32 v195, v195\n\t"
    "ds_read_b128 v[228:231], v242 offset:9264\n\t"
    "v_exp_f32 v196, v196\n\t"
    "ds_read_b128 v[232:235], v243 offset:9264\n\t"
    "v_exp_f32 v197, v197\n\t"
    "v_mfma_f32_32x32x16_bf16 v[176:191], v[208:211], v[136:139], 0\n\t"
    "v_exp_f32 v198, v198\n\t"
    "v_exp_f32 v199, v199\n\t"
    "v_exp_f32 v200, v200\n\t"
    "v_exp_f32 v201, v201\n\t"
    "v_mfma_f32_32x32x8bf16_1k v[176:191], v[212:213], v[164:165], v[176:191]\n\t"
    "v_exp_f32 v202, v202\n\t"
    "v_exp_f32 v203, v203\n\t"
    "v_exp_f32 v204, v204\n\t"
    "v_exp_f32 v205, v205\n\t"
    "v_exp_f32 v206, v206\n\t"
    "v_exp_f32 v207, v207\n\t"
    "v_smfmac_f32_32x32x32_bf16 v[16:31], v[214:217], v[192:199], v236\n\t"
    "v_exp_f32 v176, v176\n\t"
    "v_exp_f32 v177, v177\n\t"
    "v_exp_f32 v178, v178\n\t"
    "v_smfmac_f32_32x32x32_bf16 v[16:31], v[218:221], v[200:207], v236\n\t"
    "v_exp_f32 v179, v179\n\t"
    "v_exp_f32 v180, v180\n\t"
    "v_exp_f32 v181, v181\n\t"
    "v_mfma_f32_32x32x16_bf16 v[192:207], v[208:211], v[140:143], 0\n\t"
    "v_exp_f32 v182, v182\n\t"
    "v_exp_f32 v183, v183\n\t"
    "v_exp_f32 v184, v184\n\t"
    "v_exp_f32 v185, v185\n\t"
    "v_mfma_f32_32x32x8bf16_1k v[192:207], v[212:213], v[166:167], v[192:207]\n\t"
    "v_exp_f32 v186, v186\n\t"
    "v_exp_f32 v187, v187\n\t"
    "v_exp_f32 v188, v188\n\t"
    "v_exp_f32 v189, v189\n\t"
    "v_exp_f32 v190, v190\n\t"
    "v_exp_f32 v191, v191\n\t"
    "v_smfmac_f32_32x32x32_bf16 v[32:47], v[214:217], v[176:183], v236\n\t"
    "v_exp_f32 v192, v192\n\t"
    "v_add_u32 v237, %[ax0], v240\n\t"
    "v_exp_f32 v193, v193\n\t"
    "v_add_u32 v238, %[ax1], v240\n\t"
    "v_exp_f32 v194, v194\n\t"
    "v_smfmac_f32_32x32x32_bf16 v[32:47], v[218:221], v[184:191], v236\n\t"
    "v_exp_f32 v195, v195\n\t"
    "v_add_u32 v239, %[ax2], v240\n\t"
    "v_exp_f32 v196, v196\n\t"
    "v_mov_b32 v241, %[cb]\n\t"
    "v_exp_f32 v197, v197\n\t"
    "v_mfma_f32_32x32x16_bf16 v[176:191], v[208:211], v[144:147], 0\n\t"
    "v_exp_f32 v198, v198\n\t"
    "v_mov_b32 v242, 1\n\t"
    "v_exp_f32 v199, v199\n\t"
    "v_exp_f32 v200, v200\n\t"
    "v_exp_f32 v201, v201\n\t"
    "v_mfma_f32_32x32x8bf16_1k v[176:191], v[212:213], v[168:169], v[176:191]\n\t"
    "v_exp_f32 v202, v202\n\t"
    "v_exp_f32 v203, v203\n\t"
    "v_exp_f32 v204, v204\n\t"
    "v_exp_f32 v205, v205\n\t"
    "v_exp_f32 v206, v206\n\t"
    "v_exp_f32 v207, v207\n\t"
    "v_smfmac_f32_32x32x32_bf16 v[48:63], v[214:217], v[192:199], v236\n\t"
    "v_exp_f32 v176, v176\n\t"
    "v_exp_f32 v177, v177\n\t"
    "v_exp_f32 v178, v178\n\t"
    "v_smfmac_f32_32x32x32_bf16 v[48:63], v[218:221], v[200:207], v236\n\t"
    "v_exp_f32 v179, v179\n\t"
    "v_exp_f32 v180, v180\n\t"
    "v_exp_f32 v181, v181\n\t"
    "v_mfma_f32_32x32x16_bf16 v[192:207], v[208:211], v[148:151], 0\n\t"
    "v_exp_f32 v182, v182\n\t"
    "v_exp_f32 v183, v183\n\t"
    "v_exp_f32 v184, v184\n\t"
    "v_exp_f32 v185, v185\n\t"
    "v_mfma_f32_32x32x8bf16_1k v[192:207], v[212:213], v[170:171], v[192:207]\n\t"
    "v_exp_f32 v186, v186\n\t"
    "v_exp_f32 v187, v187\n\t"
    "v_exp_f32 v188, v188\n\t"
    "v_exp_f32 v189, v189\n\t"
    "v_exp_f32 v190, v190\n\t"
    "v_exp_f32 v191, v191\n\t"
    "v_smfmac_f32_32x32x32_bf16 v[64:79], v[214:217], v[176:183], v236\n\t"
    "v_exp_f32 v192, v192\n\t"
    "s_waitcnt vmcnt(0) lgkmcnt(0)\n\t"
    "v_exp_f32 v193, v193\n\t"
    "v_exp_f32 v194, v194\n\t"
    "v_smfmac_f32_32x32x32_bf16 v[64:79], v[218:221], v[184:191], v236\n\t"
    "v_exp_f32 v195, v195\n\t"
    "v_exp_f32 v196, v196\n\t"
    "v_exp_f32 v197, v197\n\t"
    "v_mfma_f32_32x32x16_bf16 v[176:191], v[208:211], v[152:155], 0\n\t"
    "v_exp_f32 v198, v198\n\t"
    "v_exp_f32 v199, v199\n\t"
    "v_exp_f32 v200, v200\n\t"
    "v_exp_f32 v201, v201\n\t"
    "v_mfma_f32_32x32x8bf16_1k v[176:191], v[212:213], v[172:173], v[176:191]\n\t"
    "v_exp_f32 v202, v202\n\t"
    "v_exp_f32 v203, v203\n\t"
    "v_exp_f32 v204, v204\n\t"
    "v_exp_f32 v205, v205\n\t"
    "v_exp_f32 v206, v206\n\t"
    "v_exp_f32 v207, v207\n\t"
    "v_smfmac_f32_32x32x32_bf16 v[80:95], v[214:217], v[192:199], v236\n\t"
    "v_exp_f32 v176, v176\n\t"
    "s_mov_b64 exec, 1\n\t"
    "ds_add_u32 v241, v242 offset:4\n\t"
    "s_mov_b64 exec, -1\n\t"
    "v_exp_f32 v177, v177\n\t"
    "v_exp_f32 v178, v178\n\t"
    "v_smfmac_f32_32x32x32_bf16 v[80:95], v[218:221], v[200:207], v236\n\t"
    "v_exp_f32 v179, v179\n\t"
    "v_exp_f32 v180, v180\n\t"
    "v_exp_f32 v181, v181\n\t"
    "v_mfma_f32_32x32x16_bf16 v[192:207], v[208:211], v[156:159], 0\n\t"
    "v_exp_f32 v182, v182\n\t"
    "v_exp_f32 v183, v183\n\t"
    "v_exp_f32 v184, v184\n\t"
    "v_exp_f32 v185, v185\n\t"
    "v_mfma_f32_32x32x8bf16_1k v[192:207], v[212:213], v[174:175], v[192:207]\n\t"
    "v_exp_f32 v186, v186\n\t"
    "v_exp_f32 v187, v187\n\t"
    "v_exp_f32 v188, v188\n\t"
    "v_exp_f32 v189, v189\n\t"
    "v_exp_f32 v190, v190\n\t"
    "v_exp_f32 v191, v191\n\t"
    "v_smfmac_f32_32x32x32_bf16 v[96:111], v[214:217], v[176:183], v236\n\t"
    "v_exp_f32 v192, v192\n\t"
    "v_exp_f32 v193, v193\n\t"
    "v_exp_f32 v194, v194\n\t"
    "v_smfmac_f32_32x32x32_bf16 v[96:111], v[218:221], v[184:191], v236\n\t"
    "v_exp_f32 v195, v195\n\t"
    "v_exp_f32 v196, v196\n\t"
    "v_exp_f32 v197, v197\n\t"
    "v_mfma_f32_32x32x16_bf16 v[176:191], v[222:225], v[136:139], 0\n\t"
    "v_exp_f32 v198, v198\n\t"
    "v_exp_f32 v199, v199\n\t"
    "v_exp_f32 v200, v200\n\t"
    "v_exp_f32 v201, v201\n\t"
    "v_mfma_f32_32x32x8bf16_1k v[176:191], v[226:227], v[164:165], v[176:191]\n\t"
    "v_exp_f32 v202, v202\n\t"
    "v_exp_f32 v203, v203\n\t"
    "v_exp_f32 v204, v204\n\t"
    "v_exp_f32 v205, v205\n\t"
    "v_exp_f32 v206, v206\n\t"
    "v_exp_f32 v207, v207\n\t"
    "v_smfmac_f32_32x32x32_bf16 v[112:127], v[214:217], v[192:199], v236\n\t"
    "v_exp_f32 v176, v176\n\t"
    "v_subrev_u32 v243, %[rk0], v240\n\t"
    "v_exp_f32 v177, v177\n\t"
    "v_mul_u32_u24 v243, 3, v243\n\t"
    "v_exp_f32 v178, v178\n\t"
    "v_smfmac_f32_32x32x32_bf16 v[112:127], v[218:221], v[200:207], v236\n\t"
    "v_exp_f32 v179, v179\n\t"
    "v_lshrrev_b32 v243, 2, v243\n\t"
    "v_exp_f32 v180, v180\n\t"
    "v_sub_u32 v208, v237, v243\n\t"
    "v_exp_f32 v181, v181\n\t"
    "v_mfma_f32_32x32x16_bf16 v[192:207], v[222:225], v[140:143], 0\n\t"
    "v_exp_f32 v182, v182\n\t"
    "v_sub_u32 v209, v238, v243\n\t"
    "v_exp_f32 v183, v183\n\t"
    "v_sub_u32 v210, v239, v243\n\t"
    "v_exp_f32 v184, v184\n\t"
    "v_exp_f32 v185, v185\n\t"
    "v_mfma_f32_32x32x8bf16_1k v[192:207], v[226:227], v[166:167], v[192:207]\n\t"
    "v_exp_f32 v186, v186\n\t"
    "v_exp_f32 v187, v187\n\t"
    "v_exp_f32 v188, v188\n\t"
    "v_exp_f32 v189, v189\n\t"
    "v_exp_f32 v190, v190\n\t"
    "v_exp_f32 v191, v191\n\t"
    "v_smfmac_f32_32x32x32_bf16 v[32:47], v[228:231], v[176:183], v236\n\t"
    "v_exp_f32 v192, v192\n\t"
    "ds_read_b32 v243, v241 offset:4\n\t"
    "v_exp_f32 v193, v193\n\t"
    "s_mov_b32 %[t2], 2097152\n\t"
    "v_exp_f32 v194, v194\n\t"
    "v_smfmac_f32_32x32x32_bf16 v[32:47], v[232:235], v[184:191], v236\n\t"
    "v_exp_f32 v195, v195\n\t"
    "v_exp_f32 v196, v196\n\t"
    "v_exp_f32 v197, v197\n\t"
    "v_mfma_f32_32x32x16_bf16 v[176:191], v[222:225], v[144:147], 0\n\t"
    "v_exp_f32 v198, v198\n\t"
    "v_exp_f32 v199, v199\n\t"
    "v_exp_f32 v200, v200\n\t"
    "v_exp_f32 v201, v201\n\t"
    "v_mfma_f32_32x32x8bf16_1k v[176:191], v[226:227], v[168:169], v[176:191]\n\t"
    "v_exp_f32 v202, v202\n\t"
    "v_exp_f32 v203, v203\n\t"
    "v_exp_f32 v204, v204\n\t"
    "v_exp_f32 v205, v205\n\t"
    "v_exp_f32 v206, v206\n\t"
    "v_exp_f32 v207, v207\n\t"
    ".LLX_%=:\n\t"
    "s_waitcnt lgkmcnt(0)\n\t"
    "s_sub_u32 %[t2], %[t2], 1\n\t"
    "v_readfirstlane_b32 %[t], v243\n\t"
    "s_cselect_b32 %[t], -1, %[t]\n\t"
    "s_cmp_lt_u32 %[t], 4\n\t"
    "s_cbranch_scc0 .LLY_%=\n\t"
    "ds_read_b32 v243, v241 offset:4\n\t"
    "s_branch .LLX_%=\n\t"
    ".LLY_%=:\n\t"
    "v_smfmac_f32_32x32x32_bf16 v[48:63], v[228:231], v[192:199], v236\n\t"
    "v_exp_f32 v176, v176\n\t"
    "ds_write_b128 v237, v[32:35] offset:0\n\t"
    "v_exp_f32 v177, v177\n\t"
    "ds_write_b128 v237, v[36:39] offset:1024\n\t"
    "v_exp_f32 v178, v178\n\t"
    "v_smfmac_f32_32x32x32_bf16 v[48:63], v[232:235], v[200:207], v236\n\t"
    "v_exp_f32 v179, v179\n\t"
    "v_exp_f32 v180, v180\n\t"
    "v_exp_f32 v181, v181\n\t"
    "v_mfma_f32_32x32x16_bf16 v[192:207], v[222:225], v[148:151], 0\n\t"
    "v_exp_f32 v182, v182\n\t"
    "v_exp_f32 v183, v183\n\t"
    "v_exp_f32 v184, v184\n\t"
    "v_exp_f32 v185, v185\n\t"
    "v_mfma_f32_32x32x8bf16_1k v[192:207], v[226:227], v[170:171], v[192:207]\n\t"
    "v_exp_f32 v186, v186\n\t"
    "v_exp_f32 v187, v187\n\t"
    "v_exp_f32 v188, v188\n\t"
    "v_exp_f32 v189, v189\n\t"
    "v_exp_f32 v190, v190\n\t"
    "v_exp_f32 v191, v191\n\t"
    "v_smfmac_f32_32x32x32_bf16 v[64:79], v[228:231], v[176:183], v236\n\t"
    "v_exp_f32 v192, v192\n\t"
    "ds_write_b128 v237, v[48:51] offset:2048\n\t"
    "v_exp_f32 v193, v193\n\t"
    "ds_write_b128 v237, v[52:55] offset:3072\n\t"
    "v_exp_f32 v194, v194\n\t"
    "v_smfmac_f32_32x32x32_bf16 v[64:79], v[232:235], v[184:191], v236\n\t"
    "v_exp_f32 v195, v195\n\t"
    "v_permlane32_swap_b32 v40, v56\n\t"
    "v_exp_f32 v196, v196\n\t"
    "v_permlane32_swap_b32 v41, v57\n\t"
    "v_exp_f32 v197, v197\n\t"
    "v_mfma_f32_32x32x16_bf16 v[176:191], v[222:225], v[152:155], 0\n\t"
    "v_exp_f32 v198, v198\n\t"
    "v_permlane32_swap_b32 v42, v58\n\t"
    "v_exp_f32 v199, v199\n\t"
    "v_permlane32_swap_b32 v43, v59\n\t"
    "v_exp_f32 v200, v200\n\t"
    "ds_write_b128 v237, v[40:43] offset:4096\n\t"
    "v_exp_f32 v201, v201\n\t"
    "v_mfma_f32_32x32x8bf16_1k v[176:191], v[226:227], v[172:173], v[176:191]\n\t"
    "v_exp_f32 v202, v202\n\t"
    "ds_write_b32 v208, v56 offset:5120\n\t"
    "v_exp_f32 v203, v203\n\t"
    "v_exp_f32 v204, v204\n\t"
    "v_exp_f32 v205, v205\n\t"
    "v_exp_f32 v206, v206\n\t"
    "v_exp_f32 v207, v207\n\t"
    "v_smfmac_f32_32x32x32_bf16 v[80:95], v[228:231], v[192:199], v236\n\t"
    "v_exp_f32 v176, v176\n\t"
    "ds_write_b128 v238, v[64:67] offset:0\n\t"
    "v_exp_f32 v177, v177\n\t"
    "ds_write_b128 v238, v[68:71] offset:1024\n\t"
    "v_exp_f32 v178, v178\n\t"
    "v_smfmac_f32_32x32x32_bf16 v[80:95], v[232:235], v[200:207], v236\n\t"
    "v_exp_f32 v179, v179\n\t"
    "v_exp_f32 v180, v180\n\t"
    "v_exp_f32 v181, v181\n\t"
    "v_mfma_f32_32x32x16_bf16 v[192:207], v[222:225], v[156:159], 0\n\t"
    "v_exp_f32 v182, v182\n\t"
    "v_exp_f32 v183, v183\n\t"
    "v_exp_f32 v184, v184\n\t"
    "v_exp_f32 v185, v185\n\t"
    "v_mfma_f32_32x32x8bf16_1k v[192:207], v[226:227], v[174:175], v[192:207]\n\t"
    "v_exp_f32 v186, v186\n\t"
    "v_exp_f32 v187, v187\n\t"
    "v_exp_f32 v188, v188\n\t"
    "v_exp_f32 v189, v189\n\t"
    "v_exp_f32 v190, v190\n\t"
    "v_exp_f32 v191, v191\n\t"
    "v_smfmac_f32_32x32x32_bf16 v[96:111], v[228:231], v[176:183], v236\n\t"
    "v_exp_f32 v192, v192\n\t"
    "ds_write_b128 v238, v[80:83] offset:2048\n\t"
    "v_exp_f32 v193, v193\n\t"
    "ds_write_b128 v238, v[84:87] offset:3072\n\t"
    "v_exp_f32 v194, v194\n\t"
    "v_smfmac_f32_32x32x32_bf16 v[96:111], v[232:235], v[184:191], v236\n\t"
    "v_exp_f32 v195, v195\n\t"
    "v_permlane32_swap_b32 v72, v88\n\t"
    "v_exp_f32 v196, v196\n\t"
    "v_permlane32_swap_b32 v73, v89\n\t"
    "v_exp_f32 v197, v197\n\t"
    "v_mfma_f32_32x32x16_bf16 v[176:191], v[222:225], v[128:131], 0\n\t"
    "v_exp_f32 v198, v198\n\t"
    "v_permlane32_swap_b32 v74, v90\n\t"
    "v_exp_f32 v199, v199\n\t"
    "v_permlane32_swap_b32 v75, v91\n\t"
    "v_exp_f32 v200, v200\n\t"
    "ds_write_b128 v238, v[72:75] offset:4096\n\t"
    "v_exp_f32 v201, v201\n\t"
    "v_mfma_f32_32x32x8bf16_1k v[176:191], v[226:227], v[160:161], v[176:191]\n\t"
    "v_exp_f32 v202, v202\n\t"
    "ds_write_b32 v209, v88 offset:5120\n\t"
    "v_exp_f32 v203, v203\n\t"
    "v_exp_f32 v204, v204\n\t"
    "v_exp_f32 v205, v205\n\t"
    "v_exp_f32 v206, v206\n\t"
    "v_exp_f32 v207, v207\n\t"
    "v_smfmac_f32_32x32x32_bf16 v[112:127], v[228:231], v[192:199], v236\n\t"
    "v_exp_f32 v176, v176\n\t"
    "ds_write_b128 v239, v[96:99] offset:0\n\t"
    "v_exp_f32 v177, v177\n\t"
    "ds_write_b128 v239, v[100:103] offset:1024\n\t"
    "v_exp_f32 v178, v178\n\t"
    "v_smfmac_f32_32x32x32_bf16 v[112:127], v[232:235], v[200:207], v236\n\t"
    "v_exp_f32 v179, v179\n\t"
    "v_exp_f32 v180, v180\n\t"
    "v_exp_f32 v181, v181\n\t"
    "v_mfma_f32_32x32x16_bf16 v[192:207], v[222:225], v[132:135], 0\n\t"
    "v_exp_f32 v182, v182\n\t"
    "v_exp_f32 v183, v183\n\t"
    "v_exp_f32 v184, v184\n\t"
    "v_exp_f32 v185, v185\n\t"
    "v_mfma_f32_32x32x8bf16_1k v[192:207], v[226:227], v[162:163], v[192:207]\n\t"
    "v_exp_f32 v186, v186\n\t"
    "v_exp_f32 v187, v187\n\t"
    "v_exp_f32 v188, v188\n\t"
    "v_exp_f32 v189, v189\n\t"
    "v_exp_f32 v190, v190\n\t"
    "v_exp_f32 v191, v191\n\t"
    "v_smfmac_f32_32x32x32_bf16 v[0:15], v[228:231], v[176:183], v236\n\t"
    "v_exp_f32 v192, v192\n\t"
    "ds_write_b128 v239, v[112:115] offset:2048\n\t"
    "v_exp_f32 v193, v193\n\t"
    "ds_write_b128 v239, v[116:119] offset:3072\n\t"
    "v_exp_f32 v194, v194\n\t"
    "v_smfmac_f32_32x32x32_bf16 v[0:15], v[232:235], v[184:191], v236\n\t"
    "v_exp_f32 v195, v195\n\t"
    "v_permlane32_swap_b32 v104, v120\n\t"
    "v_exp_f32 v196, v196\n\t"
    "v_permlane32_swap_b32 v105, v121\n\t"
    "v_exp_f32 v197, v197\n\t"
    "v_exp_f32 v198, v198\n\t"
    "v_permlane32_swap_b32 v106, v122\n\t"
    "v_exp_f32 v199, v199\n\t"
    "v_permlane32_swap_b32 v107, v123\n\t"
    "v_exp_f32 v200, v200\n\t"
    "ds_write_b128 v239, v[104:107] offset:4096\n\t"
    "v_exp_f32 v201, v201\n\t"
    "v_exp_f32 v202, v202\n\t"
    "ds_write_b32 v210, v120 offset:5120\n\t"
    "v_exp_f32 v203, v203\n\t"
    "v_exp_f32 v204, v204\n\t"
    "v_exp_f32 v205, v205\n\t"
    "v_exp_f32 v206, v206\n\t"
    "v_exp_f32 v207, v207\n\t"
    "s_nop 1\n\t"
    "v_smfmac_f32_32x32x32_bf16 v[16:31], v[228:231], v[192:199], v236\n\t"
    "v_smfmac_f32_32x32x32_bf16 v[16:31], v[232:235], v[200:207], v236\n\t"
    "v_mov_b32 v242, 1\n\t"
    "s_waitcnt lgkmcnt(0)\n\t"
    "s_mov_b64 exec, 1\n\t"
    "ds_add_u32 v241, v242 offset:16\n\t"
    "s_mov_b64 exec, -1\n\t"
    "s_mov_b32 m0, %[m0s]\n\t"
    "s_setprio 3\n\t"
    "s_nop 15\n\t"
        : "={v[0:15]}"(o[0]), "={v[16:31]}"(o[1]), "={v[32:47]}"(o[2]), "={v[48:63]}"(o[3]), "={v[64:79]}"(o[4]), "={v[80:95]}"(o[5]), "={v[96:111]}"(o[6]), "={v[112:127]}"(o[7]),
          [st] "+s"(st), [t] "=&s"(t), [m0s] "=&s"(m0s), "+{v237}"(ds[0]), "+{v238}"(ds[1]), "+{v239}"(ds[2]), "+{v240}"(rk), "+{v241}"(rk4), "+{v242}"(rv0), "+{v243}"(rv1), "+{v222}"(vg), "+{v223}"(vq), "+{v224}"(vq4), "+{v179}"(vp), [t2] "=&s"(t2), "={v244}"(gt[0]), "={v245}"(gt[1])
        : [srd] "s"(srd), [ring] "s"(ar), [cs] "s"(cS), [cb] "s"(cW),
          [ax0] "s"(axb + ((w + 1) & 3) * 16128), [ax1] "s"(axb + ((w + 2) & 3) * 16128 + 5376), [ax2] "s"(axb + ((w + 3) & 3) * 16128 + 10752), [rk0] "s"(ar + odd * 512), [tlo] "s"((unsigned)tag), [thi] "s"((unsigned)(tag >> 32)),
          [q0] "s"(qs[0]), [q1] "s"(qs[1]), [q2] "s"(qs[2]), [q3] "s"(qs[3]), [q4] "s"(qs[4]), [q5] "s"(qs[5]), [q6] "s"(qs[6]), [q7] "s"(qs[7])
        : "memory", "scc", "vcc", "v128", "v129", "v130", "v131", "v132", "v133", "v134", "v135", "v136", "v137", "v138", "v139", "v140", "v141", "v142", "v143", "v144", "v145", "v146", "v147", "v148", "v149", "v150", "v151", "v152", "v153", "v154", "v155", "v156", "v157", "v158", "v159", "v160", "v161", "v162", "v163", "v164", "v165", "v166", "v167", "v168", "v169", "v170", "v171", "v172", "v173", "v174", "v175", "v176", "v177", "v178", "v180", "v181", "v182", "v183", "v184", "v185", "v186", "v187", "v188", "v189", "v190", "v191", "v192", "v193", "v194", "v195", "v196", "v197", "v198", "v199", "v200", "v201", "v202", "v203", "v204", "v205", "v206", "v207", "v208", "v209", "v210", "v211", "v212", "v213", "v214", "v215", "v216", "v217", "v218", "v219", "v220", "v221", "v225", "v226", "v227", "v228", "v229", "v230", "v231", "v232", "v233", "v234", "v235", "v236"); }
    asm volatile("" : "+v"(lane));
    const int sg = 2 * head + (lane >> 5), s1 = 64 + hp; const unsigned ob = WS_O + rbo * 81920 + ((lane >> 4) & 1) * 1024;
    unsigned oa0 = ob + (lane & 15) * 16 + (sg >> 2) * 4096 + (sg & 3) * 256, oa1 = ob + (lane >> 5) * 2048 + (s1 >> 2) * 4096 + odd * 512 + (s1 & 3) * 128 + (lane & 15) * 8;
    unsigned xa = L_AX + w * 16128 + lane * 16, xl = L_AX + w * 16128 + 5120 + lane * 4;
    f32x4 r[2][2], r2, in[3][5]; float lp, li[3], gp;
#pragma unroll
    for (int c = 0; c < 4; ++c) { const auto t = __builtin_amdgcn_permlane32_swap(__float_as_uint(o[0][8 + c]), __float_as_uint(o[1][8 + c]), false, false);
      r2[c] = __uint_as_float(t[0]); if (c == 0) lp = __uint_as_float(t[1]); }
    gp = lane < 32 ? gt[0] : gt[1];
    asm volatile("" : "+v"(oa0), "+v"(oa1), "+v"(xa), "+v"(xl), "+v"(r2), "+v"(lp), "+v"(gp));
    lds_wait4(cA);
#pragma unroll
    for (int i = 0; i < 3; ++i) { li[i] = *(const LAS float*)(lds + xl + i * 5376);
#pragma unroll
      for (int c = 0; c < 5; ++c) in[i][c] = *(const LAS f32x4*)(lds + xa + i * 5376 + c * 1024); }
    SBAR();
    lp = (lp + li[0]) + (li[1] + li[2]); r2 = (r2 + in[0][4]) + (in[1][4] + in[2][4]);
    f32x4 nf = r2 * 0.f;
#pragma unroll
    for (int s = 0; s < 2; ++s)
#pragma unroll
      for (int c = 0; c < 2; ++c) { r[s][c] = (f32x4{o[s][4 * c], o[s][4 * c + 1], o[s][4 * c + 2], o[s][4 * c + 3]} + in[0][2 * s + c]) + (in[1][2 * s + c] + in[2][2 * s + c]);
        nf = __builtin_elementwise_fma(r[s][c], f32x4{}, nf); }
    const bool safe = lp >= 0x1p-60f && lp <= 0x1p60f && (nf[0] + nf[1]) + (nf[2] + nf[3]) == 0.f;
    if (__builtin_expect(!__all(safe), 0)) {
      const unsigned vk = lane * 16 + odd * 1536, vk4 = (lane & 31) * 16 + odd * 8, vv = (lane >> 5) * 10240 + min(lane & 31, 19) * 16;
      const f32x16 t0 = attn_safe(rws, qo + 2 * w * 40960, ko, vo, vk, vk4, vv, lane), t1 = attn_safe(rws, qo + (2 * w + 1) * 40960, ko, vo, vk, vk4, vv, lane);
#pragma unroll
      for (int c = 0; c < 4; ++c) { const auto t = __builtin_amdgcn_permlane32_swap(__float_as_uint(t0[8 + c]), __float_as_uint(t1[8 + c]), false, false);
        r2[c] = __uint_as_float(t[0]); if (c == 0) lp = __uint_as_float(t[1]);
        r[0][0][c] = t0[c]; r[0][1][c] = t0[4 + c]; r[1][0][c] = t1[c]; r[1][1][c] = t1[4 + c]; } }
    { const float sp = gp * __builtin_amdgcn_rcpf(lp);
      const auto t = __builtin_amdgcn_permlane32_swap(__float_as_uint(sp), __float_as_uint(sp), false, false);
#pragma unroll
      for (int s = 0; s < 2; ++s) { const float sc = __uint_as_float(t[s]); st16(pk8(r[s][0] * sc, r[s][1] * sc), rws, oa0 + s * 2048); }
      st8(pk4(r2 * sp), rws, oa1); }
    publish(ws, WS_FO + (rbo * 32 + head) * 128, tag, lane);
    if (b == 0 && w == 0) {
#pragma nounroll
      for (int i = 0; i < 4; ++i) poll64(srd, WS_FQ + (64 * i + lane) * 128, tag);
      if (lane == 0) __hip_atomic_store((GAS u64*)(ws + WS_EP), tag, RLX_AGENT); }
    return;
  }
  const int u0 = z_u0(g), nu = z_nu(g), i16 = lane & 15, fq = lane >> 4;
  __builtin_amdgcn_s_setprio(2);
  const int P = w < 3 ? ((u0 + 1) >> 1) + w : (u0 & 1) ? (u0 >> 1) : (u0 >> 1) + 3;
  const unsigned pa = rb * (27 * 4096) + P * 4096 + (w < 3 ? lane * 16 : (u0 & 1) * 512 + lane * 8);
  constexpr unsigned OOB = 0x7fff0000u;
  u32x4 dd[6] = {}; u32x2 de[2] = {}; unsigned dv[8], dflag = OOB;
#pragma unroll
  for (int i = 0; i < 8; ++i) dv[i] = OOB;
  if (!(bi & 1)) { const int task = (b >> 4) * 32 + (b & 7) * 4 + w;
    if (task < 376) { wf_task(task, wo, wout, lds, lds0, ring, lane, dd); dflag = WS_FW + ((task & 7) * 48 + (task >> 3)) * 128;
#pragma unroll
      for (int j = 0; j < 5; ++j) dv[j] = WS_WF + (task * 5 + j) * 1024 + lane * 16; }
  } else { const int rb1 = (xcd >> 2) * 8 + (bi >> 2), g1 = 2 * (xcd & 3) + ((bi >> 1) & 1), v0 = z_u0(g1);
    const unsigned base = WS_Z1 + (2 * rb1 + (w >> 1)) * (27 * 4096) + (2 * (w & 1)) * 1024;
#pragma unroll
    for (int i = 0; i < 6; ++i) { dd[i] = *(const LAS u32x4*)(lds + L_WR + w * 24576 + 16384 + i * 1024 + lane * 16); dv[i] = base + (((v0 + 1) >> 1) + (i >> 1)) * 4096 + (i & 1) * 1024 + lane * 16; }
#pragma unroll
    for (int m = 0; m < 2; ++m) { de[m] = *(const LAS u32x2*)(lds + L_WR + w * 24576 + 16384 + (6 + m) * 1024 + lane * 16);
      if (z_nu(g1) == 7) dv[6 + m] = base + ((v0 & 1) ? (v0 >> 1) * 4096 + 512 : ((v0 >> 1) + 3) * 4096) + m * 1024 + lane * 8; }
    dflag = WS_FZ1 + (g1 * 64 + 4 * rb1 + w) * 128; }
  f32x16 ac[7]; u32x4 zf[4]; f32x4 gm; bf16x8 wf[7][5], uf[7][4];
  { unsigned vz[7]; typedef unsigned u32x16 __attribute__((ext_vector_type(16))); typedef unsigned u32x12 __attribute__((ext_vector_type(12))); u32x16 zq, wq0, wq2, wq3; u32x12 wq1;
    const rsrc_t rbs = mk_rsrc(bsp, 4096);
#pragma unroll
    for (int m = 0; m < 4; ++m) { const float bs = bflo(__builtin_amdgcn_raw_buffer_load_b16(rbs, (64 * rb + 16 * m + i16) * 2, 0, 0));
#pragma unroll
      for (int r = 0; r < 4; ++r) { ac[0][4 * m + r] = ac[1][4 * m + r] = w < 3 ? bs : 0.f; ac[6][4 * m + r] = w < 3 ? 0.f : bs; } }
#pragma unroll
    for (int e = 0; e < 16; ++e) { ac[4][e] = __uint_as_float(dd[e >> 2][e & 3]); ac[5][e] = __uint_as_float(e < 8 ? dd[4 + (e >> 2)][e & 3] : e < 12 ? de[(e - 8) >> 1][e & 1] : 0u);
      ac[3][e] = __uint_as_float(e < 8 ? 0u : dv[e - 8]); }
    unsigned vc = lane < 2 ? WS_FZ1 + (g * 64 + 2 * rb + lane) * 128 : lane < 3 ? WS_FQ + (7 * 16 + (rb >> 1)) * 128
                      : WS_FW + (g * 48 + (lane < 10 ? min(7 * w + lane - 3, 26) : 27 + (lane < 14 ? 4 * (lane - 10) + w : 16 + w))) * 128;
#pragma unroll
    for (int j = 0; j < 7; ++j) { int pr = (j >> 1) + (w < 3 ? w : 0); pr -= pr >= 3 ? 3 : 0;
      const int U = j < 6 ? u0 + (u0 & 1) + 2 * pr + (j & 1) : (u0 & 1) ? u0 : u0 + 6; vz[j] = (j == 6 && nu == 6) ? 0x7fff0000u : U * 1024 + lane * 16; }
    const int r8 = lane >> 3;
    unsigned vw0 = r8 * 4096 + (((lane & 7) ^ (r8 >> 1)) * 16), vw1 = r8 * 4096 + (((lane & 7) ^ (4 + (r8 >> 1))) * 16);
    unsigned vr0 = ring + (i16 >> 3) * 1024 + (i16 & 7) * 128 + ((fq ^ (i16 >> 1)) * 16), vr1 = vr0 ^ 64;
    unsigned sz = WS_ZT + 16 * w * 55296, swd = sw + 128, t, m0s;
    unsigned vpa = pa, vgt = WS_GATE + (64 * rb + i16) * 4, vbs = lane * 16, swf = WS_WF + g * 5120 + 7 * w * 40960, vfl = lane == 0 ? dflag : OOB;
    asm volatile("" : "+{v[0:15]}"(ac[0]), "+{v[16:31]}"(ac[1]), "+{v[48:63]}"(ac[3]), "+{v[64:79]}"(ac[4]), "+{v[80:95]}"(ac[5]), "+{v[96:111]}"(ac[6]), "+{v228}"(vz[0]), "+{v229}"(vz[1]), "+{v230}"(vz[2]), "+{v231}"(vz[3]), "+{v232}"(vz[4]), "+{v233}"(vz[5]), "+{v234}"(vz[6]),
                      "+{v235}"(vw0), "+{v236}"(vw1), "+{v237}"(vr0), "+{v238}"(vr1), "+{v239}"(vc), "+{v244}"(vpa), "+{v245}"(vgt), "+{v246}"(vbs), "+{v247}"(vfl));
  lds_wait4<2>(cS, 256);
  wring_prime(ring, srw, sw, vw0, vw1);
  poll64(srd, WS_FZT + (g * 16 + 4 * w + (lane & 3)) * 128, tag);
    __builtin_amdgcn_s_setprio(0);
    asm volatile(
    "s_nop 4\n\t"
    "s_mov_b32 %[m0s], m0\n\t"
    "buffer_load_dwordx4 v[168:171], v228, %[srd], %[sz] offen sc1\n\t"
    "buffer_load_dwordx4 v[172:175], v229, %[srd], %[sz] offen sc1\n\t"
    "buffer_load_dwordx4 v[176:179], v230, %[srd], %[sz] offen sc1\n\t"
    "buffer_load_dwordx4 v[180:183], v231, %[srd], %[sz] offen sc1\n\t"
    "buffer_load_dwordx4 v[184:187], v232, %[srd], %[sz] offen sc1\n\t"
    "buffer_load_dwordx4 v[188:191], v233, %[srd], %[sz] offen sc1\n\t"
    "buffer_load_dwordx4 v[192:195], v234, %[srd], %[sz] offen sc1\n\t"
    "s_add_u32 %[sz], %[sz], 55296\n\t"
    "buffer_load_dwordx4 v[112:115], v228, %[srd], %[sz] offen sc1\n\t"
    "buffer_load_dwordx4 v[116:119], v229, %[srd], %[sz] offen sc1\n\t"
    "buffer_load_dwordx4 v[120:123], v230, %[srd], %[sz] offen sc1\n\t"
    "buffer_load_dwordx4 v[124:127], v231, %[srd], %[sz] offen sc1\n\t"
    "buffer_load_dwordx4 v[128:131], v232, %[srd], %[sz] offen sc1\n\t"
    "buffer_load_dwordx4 v[132:135], v233, %[srd], %[sz] offen sc1\n\t"
    "buffer_load_dwordx4 v[136:139], v234, %[srd], %[sz] offen sc1\n\t"
    "s_add_u32 %[sz], %[sz], 55296\n\t"
    "buffer_load_dwordx4 v[140:143], v228, %[srd], %[sz] offen sc1\n\t"
    "buffer_load_dwordx4 v[144:147], v229, %[srd], %[sz] offen sc1\n\t"
    "buffer_load_dwordx4 v[148:151], v230, %[srd], %[sz] offen sc1\n\t"
    "buffer_load_dwordx4 v[152:155], v231, %[srd], %[sz] offen sc1\n\t"
    "buffer_load_dwordx4 v[156:159], v232, %[srd], %[sz] offen sc1\n\t"
    "buffer_load_dwordx4 v[160:163], v233, %[srd], %[sz] offen sc1\n\t"
    "buffer_load_dwordx4 v[164:167], v234, %[srd], %[sz] offen sc1\n\t"
    "buffer_store_dwordx4 v[64:67], v56, %[srd], 0 offen sc1\n\t"
    "buffer_store_dwordx4 v[68:71], v57, %[srd], 0 offen sc1\n\t"
    "buffer_store_dwordx4 v[72:75], v58, %[srd], 0 offen sc1\n\t"
    "buffer_store_dwordx4 v[76:79], v59, %[srd], 0 offen sc1\n\t"
    "buffer_store_dwordx4 v[80:83], v60, %[srd], 0 offen sc1\n\t"
    "buffer_store_dwordx4 v[84:87], v61, %[srd], 0 offen sc1\n\t"
    "buffer_store_dwordx2 v[88:89], v62, %[srd], 0 offen sc1\n\t"
    "buffer_store_dwordx2 v[90:91], v63, %[srd], 0 offen sc1\n\t"
    "v_mov_b32 v242, %[tlo]\n\t"
    "v_mov_b32 v243, %[thi]\n\t"
    "ds_read_b128 v[196:199], v237 offset:0\n\t"
    "ds_read_b128 v[200:203], v237 offset:2048\n\t"
    "ds_read_b128 v[204:207], v237 offset:4096\n\t"
    "ds_read_b128 v[208:211], v237 offset:6144\n\t"
    "s_waitcnt lgkmcnt(0)\n\t"
    "s_waitcnt vmcnt(28)\n\t"
    "s_setprio 1\n\t"
    "v_mfma_f32_16x16x32_bf16 v[0:3], v[168:171], v[196:199], v[0:3]\n\t"
    "v_mfma_f32_16x16x32_bf16 v[4:7], v[168:171], v[200:203], v[4:7]\n\t"
    "s_setprio 0\n\t"
    "v_mfma_f32_16x16x32_bf16 v[8:11], v[168:171], v[204:207], v[8:11]\n\t"
    "s_setprio 1\n\t"
    "v_mfma_f32_16x16x32_bf16 v[12:15], v[168:171], v[208:211], v[12:15]\n\t"
    "s_add_u32 %[sz], %[sz], 55296\n\t"
    "buffer_load_dwordx4 v[168:171], v228, %[srd], %[sz] offen sc1\n\t"
    "s_add_u32 %[swd], %[swd], 128\n\t"
    "s_add_u32 m0, %[ring], 16384\n\t"
    "s_add_u32 %[t], %[swd], 0\n\t"
    "buffer_load_dwordx4 v235, %[srw], %[t] offen lds\n\t"
    "s_waitcnt vmcnt(29)\n\t"
    "v_mfma_f32_16x16x32_bf16 v[16:19], v[172:175], v[196:199], v[16:19]\n\t"
    "s_setprio 0\n\t"
    "v_mfma_f32_16x16x32_bf16 v[20:23], v[172:175], v[200:203], v[20:23]\n\t"
    "s_setprio 1\n\t"
    "v_mfma_f32_16x16x32_bf16 v[24:27], v[172:175], v[204:207], v[24:27]\n\t"
    "v_mfma_f32_16x16x32_bf16 v[28:31], v[172:175], v[208:211], v[28:31]\n\t"
    "buffer_load_dwordx4 v[172:175], v229, %[srd], %[sz] offen sc1\n\t"
    "s_add_u32 m0, %[ring], 17408\n\t"
    "s_add_u32 %[t], %[swd], 32768\n\t"
    "buffer_load_dwordx4 v236, %[srw], %[t] offen lds\n\t"
    "s_waitcnt vmcnt(30)\n\t"
    "s_setprio 0\n\t"
    "v_mfma_f32_16x16x32_bf16 v[32:35], v[176:179], v[196:199], 0\n\t"
    "s_setprio 1\n\t"
    "v_mfma_f32_16x16x32_bf16 v[36:39], v[176:179], v[200:203], 0\n\t"
    "v_mfma_f32_16x16x32_bf16 v[40:43], v[176:179], v[204:207], 0\n\t"
    "s_setprio 0\n\t"
    "v_mfma_f32_16x16x32_bf16 v[44:47], v[176:179], v[208:211], 0\n\t"
    "s_setprio 1\n\t"
    "buffer_load_dwordx4 v[176:179], v230, %[srd], %[sz] offen sc1\n\t"
    "s_add_u32 m0, %[ring], 18432\n\t"
    "s_add_u32 %[t], %[swd], 65536\n\t"
    "buffer_load_dwordx4 v235, %[srw], %[t] offen lds\n\t"
    "ds_read_b128 v[212:215], v238 offset:0\n\t"
    "s_waitcnt vmcnt(31)\n\t"
    "v_mfma_f32_16x16x32_bf16 v[48:51], v[180:183], v[196:199], 0\n\t"
    "v_mfma_f32_16x16x32_bf16 v[52:55], v[180:183], v[200:203], 0\n\t"
    "s_setprio 0\n\t"
    "v_mfma_f32_16x16x32_bf16 v[56:59], v[180:183], v[204:207], 0\n\t"
    "s_setprio 1\n\t"
    "v_mfma_f32_16x16x32_bf16 v[60:63], v[180:183], v[208:211], 0\n\t"
    "buffer_load_dwordx4 v[180:183], v231, %[srd], %[sz] offen sc1\n\t"
    "s_add_u32 m0, %[ring], 19456\n\t"
    "s_add_u32 %[t], %[swd], 98304\n\t"
    "buffer_load_dwordx4 v236, %[srw], %[t] offen lds\n\t"
    "ds_read_b128 v[216:219], v238 offset:2048\n\t"
    "s_waitcnt vmcnt(32)\n\t"
    "v_mfma_f32_16x16x32_bf16 v[64:67], v[184:187], v[196:199], 0\n\t"
    "s_setprio 0\n\t"
    "v_mfma_f32_16x16x32_bf16 v[68:71], v[184:187], v[200:203], 0\n\t"
    "s_setprio 1\n\t"
    "v_mfma_f32_16x16x32_bf16 v[72:75], v[184:187], v[204:207], 0\n\t"
    "v_mfma_f32_16x16x32_bf16 v[76:79], v[184:187], v[208:211], 0\n\t"
    "buffer_load_dwordx4 v[184:187], v232, %[srd], %[sz] offen sc1\n\t"
    "s_add_u32 m0, %[ring], 20480\n\t"
    "s_add_u32 %[t], %[swd], 131072\n\t"
    "buffer_load_dwordx4 v235, %[srw], %[t] offen lds\n\t"
    "ds_read_b128 v[220:223], v238 offset:4096\n\t"
    "s_waitcnt vmcnt(33)\n\t"
    "s_setprio 0\n\t"
    "v_mfma_f32_16x16x32_bf16 v[80:83], v[188:191], v[196:199], 0\n\t"
    "s_setprio 1\n\t"
    "v_mfma_f32_16x16x32_bf16 v[84:87], v[188:191], v[200:203], 0\n\t"
    "v_mfma_f32_16x16x32_bf16 v[88:91], v[188:191], v[204:207], 0\n\t"
    "s_setprio 0\n\t"
    "v_mfma_f32_16x16x32_bf16 v[92:95], v[188:191], v[208:211], 0\n\t"
    "s_setprio 1\n\t"
    "buffer_load_dwordx4 v[188:191], v233, %[srd], %[sz] offen sc1\n\t"
    "s_add_u32 m0, %[ring], 21504\n\t"
    "s_add_u32 %[t], %[swd], 163840\n\t"
    "buffer_load_dwordx4 v236, %[srw], %[t] offen lds\n\t"
    "ds_read_b128 v[224:227], v238 offset:6144\n\t"
    "s_waitcnt vmcnt(34)\n\t"
    "v_mfma_f32_16x16x32_bf16 v[96:99], v[192:195], v[196:199], v[96:99]\n\t"
    "v_mfma_f32_16x16x32_bf16 v[100:103], v[192:195], v[200:203], v[100:103]\n\t"
    "s_setprio 0\n\t"
    "v_mfma_f32_16x16x32_bf16 v[104:107], v[192:195], v[204:207], v[104:107]\n\t"
    "s_setprio 1\n\t"
    "v_mfma_f32_16x16x32_bf16 v[108:111], v[192:195], v[208:211], v[108:111]\n\t"
    "buffer_load_dwordx4 v[192:195], v234, %[srd], %[sz] offen sc1\n\t"
    "s_add_u32 m0, %[ring], 22528\n\t"
    "s_add_u32 %[t], %[swd], 196608\n\t"
    "buffer_load_dwordx4 v235, %[srw], %[t] offen lds\n\t"
    "s_add_u32 m0, %[ring], 23552\n\t"
    "s_add_u32 %[t], %[swd], 229376\n\t"
    "buffer_load_dwordx4 v236, %[srw], %[t] offen lds\n\t"
    "s_waitcnt lgkmcnt(0)\n\t"
    "s_waitcnt vmcnt(36)\n\t"
    "v_mfma_f32_16x16x32_bf16 v[0:3], v[112:115], v[212:215], v[0:3]\n\t"
    "s_setprio 0\n\t"
    "v_mfma_f32_16x16x32_bf16 v[4:7], v[112:115], v[216:219], v[4:7]\n\t"
    "s_setprio 1\n\t"
    "v_mfma_f32_16x16x32_bf16 v[8:11], v[112:115], v[220:223], v[8:11]\n\t"
    "v_mfma_f32_16x16x32_bf16 v[12:15], v[112:115], v[224:227], v[12:15]\n\t"
    "s_add_u32 %[sz], %[sz], 55296\n\t"
    "buffer_load_dwordx4 v[112:115], v228, %[srd], %[sz] offen sc1\n\t"
    "s_waitcnt vmcnt(36)\n\t"
    "s_setprio 0\n\t"
    "v_mfma_f32_16x16x32_bf16 v[16:19], v[116:119], v[212:215], v[16:19]\n\t"
    "s_setprio 1\n\t"
    "v_mfma_f32_16x16x32_bf16 v[20:23], v[116:119], v[216:219], v[20:23]\n\t"
    "v_mfma_f32_16x16x32_bf16 v[24:27], v[116:119], v[220:223], v[24:27]\n\t"
    "s_setprio 0\n\t"
    "v_mfma_f32_16x16x32_bf16 v[28:31], v[116:119], v[224:227], v[28:31]\n\t"
    "s_setprio 1\n\t"
    "buffer_load_dwordx4 v[116:119], v229, %[srd], %[sz] offen sc1\n\t"
    "s_waitcnt vmcnt(36)\n\t"
    "v_mfma_f32_16x16x32_bf16 v[32:35], v[120:123], v[212:215], v[32:35]\n\t"
    "v_mfma_f32_16x16x32_bf16 v[36:39], v[120:123], v[216:219], v[36:39]\n\t"
    "s_setprio 0\n\t"
    "v_mfma_f32_16x16x32_bf16 v[40:43], v[120:123], v[220:223], v[40:43]\n\t"
    "s_setprio 1\n\t"
    "v_mfma_f32_16x16x32_bf16 v[44:47], v[120:123], v[224:227], v[44:47]\n\t"
    "buffer_load_dwordx4 v[120:123], v230, %[srd], %[sz] offen sc1\n\t"
    "ds_read_b128 v[196:199], v237 offset:8192\n\t"
    "s_waitcnt vmcnt(36)\n\t"
    "v_mfma_f32_16x16x32_bf16 v[48:51], v[124:127], v[212:215], v[48:51]\n\t"
    "s_setprio 0\n\t"
    "v_mfma_f32_16x16x32_bf16 v[52:55], v[124:127], v[216:219], v[52:55]\n\t"
    "s_setprio 1\n\t"
    "v_mfma_f32_16x16x32_bf16 v[56:59], v[124:127], v[220:223], v[56:59]\n\t"
    "v_mfma_f32_16x16x32_bf16 v[60:63], v[124:127], v[224:227], v[60:63]\n\t"
    "buffer_load_dwordx4 v[124:127], v231, %[srd], %[sz] offen sc1\n\t"
    "ds_read_b128 v[200:203], v237 offset:10240\n\t"
    "s_waitcnt vmcnt(36)\n\t"
    "s_setprio 0\n\t"
    "v_mfma_f32_16x16x32_bf16 v[64:67], v[128:131], v[212:215], v[64:67]\n\t"
    "s_setprio 1\n\t"
    "v_mfma_f32_16x16x32_bf16 v[68:71], v[128:131], v[216:219], v[68:71]\n\t"
    "v_mfma_f32_16x16x32_bf16 v[72:75], v[128:131], v[220:223], v[72:75]\n\t"
    "s_setprio 0\n\t"
    "v_mfma_f32_16x16x32_bf16 v[76:79], v[128:131], v[224:227], v[76:79]\n\t"
    "s_setprio 1\n\t"
    "buffer_load_dwordx4 v[128:131], v232, %[srd], %[sz] offen sc1\n\t"
    "ds_read_b128 v[204:207], v237 offset:12288\n\t"
    "s_waitcnt vmcnt(36)\n\t"
    "v_mfma_f32_16x16x32_bf16 v[80:83], v[132:135], v[212:215], v[80:83]\n\t"
    "v_mfma_f32_16x16x32_bf16 v[84:87], v[132:135], v[216:219], v[84:87]\n\t"
    "s_setprio 0\n\t"
    "v_mfma_f32_16x16x32_bf16 v[88:91], v[132:135], v[220:223], v[88:91]\n\t"
    "s_setprio 1\n\t"
    "v_mfma_f32_16x16x32_bf16 v[92:95], v[132:135], v[224:227], v[92:95]\n\t"
    "buffer_load_dwordx4 v[132:135], v233, %[srd], %[sz] offen sc1\n\t"
    "ds_read_b128 v[208:211], v237 offset:14336\n\t"
    "s_waitcnt vmcnt(36)\n\t"
    "v_mfma_f32_16x16x32_bf16 v[96:99], v[136:139], v[212:215], v[96:99]\n\t"
    "s_setprio 0\n\t"
    "v_mfma_f32_16x16x32_bf16 v[100:103], v[136:139], v[216:219], v[100:103]\n\t"
    "s_setprio 1\n\t"
    "v_mfma_f32_16x16x32_bf16 v[104:107], v[136:139], v[220:223], v[104:107]\n\t"
    "v_mfma_f32_16x16x32_bf16 v[108:111], v[136:139], v[224:227], v[108:111]\n\t"
    "buffer_load_dwordx4 v[136:139], v234, %[srd], %[sz] offen sc1\n\t"
    "s_waitcnt lgkmcnt(0)\n\t"
    "s_waitcnt vmcnt(36)\n\t"
    "s_setprio 0\n\t"
    "v_mfma_f32_16x16x32_bf16 v[0:3], v[140:143], v[196:199], v[0:3]\n\t"
    "s_setprio 1\n\t"
    "v_mfma_f32_16x16x32_bf16 v[4:7], v[140:143], v[200:203], v[4:7]\n\t"
    "v_mfma_f32_16x16x32_bf16 v[8:11], v[140:143], v[204:207], v[8:11]\n\t"
    "s_setprio 0\n\t"
    "v_mfma_f32_16x16x32_bf16 v[12:15], v[140:143], v[208:211], v[12:15]\n\t"
    "s_setprio 1\n\t"
    "s_add_u32 %[sz], %[sz], 55296\n\t"
    "buffer_load_dwordx4 v[140:143], v228, %[srd], %[sz] offen sc1\n\t"
    "s_add_u32 %[swd], %[swd], 128\n\t"
    "s_add_u32 m0, %[ring], 0\n\t"
    "s_add_u32 %[t], %[swd], 0\n\t"
    "buffer_load_dwordx4 v235, %[srw], %[t] offen lds\n\t"
    "s_waitcnt vmcnt(37)\n\t"
    "v_mfma_f32_16x16x32_bf16 v[16:19], v[144:147], v[196:199], v[16:19]\n\t"
    "v_mfma_f32_16x16x32_bf16 v[20:23], v[144:147], v[200:203], v[20:23]\n\t"
    "s_setprio 0\n\t"
    "v_mfma_f32_16x16x32_bf16 v[24:27], v[144:147], v[204:207], v[24:27]\n\t"
    "s_setprio 1\n\t"
    "v_mfma_f32_16x16x32_bf16 v[28:31], v[144:147], v[208:211], v[28:31]\n\t"
    "buffer_load_dwordx4 v[144:147], v229, %[srd], %[sz] offen sc1\n\t"
    "s_add_u32 m0, %[ring], 1024\n\t"
    "s_add_u32 %[t], %[swd], 32768\n\t"
    "buffer_load_dwordx4 v236, %[srw], %[t] offen lds\n\t"
    "s_waitcnt vmcnt(38)\n\t"
    "v_mfma_f32_16x16x32_bf16 v[32:35], v[148:151], v[196:199], v[32:35]\n\t"
    "s_setprio 0\n\t"
    "v_mfma_f32_16x16x32_bf16 v[36:39], v[148:151], v[200:203], v[36:39]\n\t"
    "s_setprio 1\n\t"
    "v_mfma_f32_16x16x32_bf16 v[40:43], v[148:151], v[204:207], v[40:43]\n\t"
    "v_mfma_f32_16x16x32_bf16 v[44:47], v[148:151], v[208:211], v[44:47]\n\t"
    "buffer_load_dwordx4 v[148:151], v230, %[srd], %[sz] offen sc1\n\t"
    "s_add_u32 m0, %[ring], 2048\n\t"
    "s_add_u32 %[t], %[swd], 65536\n\t"
    "buffer_load_dwordx4 v235, %[srw], %[t] offen lds\n\t"
    "ds_read_b128 v[212:215], v238 offset:8192\n\t"
    "s_waitcnt vmcnt(39)\n\t"
    "s_setprio 0\n\t"
    "v_mfma_f32_16x16x32_bf16 v[48:51], v[152:155], v[196:199], v[48:51]\n\t"
    "s_setprio 1\n\t"
    "v_mfma_f32_16x16x32_bf16 v[52:55], v[152:155], v[200:203], v[52:55]\n\t"
    "v_mfma_f32_16x16x32_bf16 v[56:59], v[152:155], v[204:207], v[56:59]\n\t"
    "s_setprio 0\n\t"
    "v_mfma_f32_16x16x32_bf16 v[60:63], v[152:155], v[208:211], v[60:63]\n\t"
    "s_setprio 1\n\t"
    "buffer_load_dwordx4 v[152:155], v231, %[srd], %[sz] offen sc1\n\t"
    "s_add_u32 m0, %[ring], 3072\n\t"
    "s_add_u32 %[t], %[swd], 98304\n\t"
    "buffer_load_dwordx4 v236, %[srw], %[t] offen lds\n\t"
    "ds_read_b128 v[216:219], v238 offset:10240\n\t"
    "s_waitcnt vmcnt(40)\n\t"
    "v_mfma_f32_16x16x32_bf16 v[64:67], v[156:159], v[196:199], v[64:67]\n\t"
    "v_mfma_f32_16x16x32_bf16 v[68:71], v[156:159], v[200:203], v[68:71]\n\t"
    "s_setprio 0\n\t"
    "v_mfma_f32_16x16x32_bf16 v[72:75], v[156:159], v[204:207], v[72:75]\n\t"
    "s_setprio 1\n\t"
    "v_mfma_f32_16x16x32_bf16 v[76:79], v[156:159], v[208:211], v[76:79]\n\t"
    "buffer_load_dwordx4 v[156:159], v232, %[srd], %[sz] offen sc1\n\t"
    "s_add_u32 m0, %[ring], 4096\n\t"
    "s_add_u32 %[t], %[swd], 131072\n\t"
    "buffer_load_dwordx4 v235, %[srw], %[t] offen lds\n\t"
    "ds_read_b128 v[220:223], v238 offset:12288\n\t"
    "s_waitcnt vmcnt(41)\n\t"
    "v_mfma_f32_16x16x32_bf16 v[80:83], v[160:163], v[196:199], v[80:83]\n\t"
    "s_setprio 0\n\t"
    "v_mfma_f32_16x16x32_bf16 v[84:87], v[160:163], v[200:203], v[84:87]\n\t"
    "s_setprio 1\n\t"
    "v_mfma_f32_16x16x32_bf16 v[88:91], v[160:163], v[204:207], v[88:91]\n\t"
    "v_mfma_f32_16x16x32_bf16 v[92:95], v[160:163], v[208:211], v[92:95]\n\t"
    "buffer_load_dwordx4 v[160:163], v233, %[srd], %[sz] offen sc1\n\t"
    "s_add_u32 m0, %[ring], 5120\n\t"
    "s_add_u32 %[t], %[swd], 163840\n\t"
    "buffer_load_dwordx4 v236, %[srw], %[t] offen lds\n\t"
    "ds_read_b128 v[224:227], v238 offset:14336\n\t"
    "s_waitcnt vmcnt(42)\n\t"
    "s_setprio 0\n\t"
    "v_mfma_f32_16x16x32_bf16 v[96:99], v[164:167], v[196:199], v[96:99]\n\t"
    "s_setprio 1\n\t"
    "v_mfma_f32_16x16x32_bf16 v[100:103], v[164:167], v[200:203], v[100:103]\n\t"
    "v_mfma_f32_16x16x32_bf16 v[104:107], v[164:167], v[204:207], v[104:107]\n\t"
    "s_setprio 0\n\t"
    "v_mfma_f32_16x16x32_bf16 v[108:111], v[164:167], v[208:211], v[108:111]\n\t"
    "s_setprio 1\n\t"
    "buffer_load_dwordx4 v[164:167], v234, %[srd], %[sz] offen sc1\n\t"
    "s_add_u32 m0, %[ring], 6144\n\t"
    "s_add_u32 %[t], %[swd], 196608\n\t"
    "buffer_load_dwordx4 v235, %[srw], %[t] offen lds\n\t"
    "s_add_u32 m0, %[ring], 7168\n\t"
    "s_add_u32 %[t], %[swd], 229376\n\t"
    "buffer_load_dwordx4 v236, %[srw], %[t] offen lds\n\t"
    "s_waitcnt lgkmcnt(0)\n\t"
    "s_waitcnt vmcnt(36)\n\t"
    "buffer_store_dwordx2 v[242:243], v247, %[srd], 0 offen sc1\n\t"
    "v_mfma_f32_16x16x32_bf16 v[0:3], v[168:171], v[212:215], v[0:3]\n\t"
    "v_mfma_f32_16x16x32_bf16 v[4:7], v[168:171], v[216:219], v[4:7]\n\t"
    "s_setprio 0\n\t"
    "v_mfma_f32_16x16x32_bf16 v[8:11], v[168:171], v[220:223], v[8:11]\n\t"
    "s_setprio 1\n\t"
    "v_mfma_f32_16x16x32_bf16 v[12:15], v[168:171], v[224:227], v[12:15]\n\t"
    "s_add_u32 %[sz], %[sz], 55296\n\t"
    "buffer_load_dwordx4 v[168:171], v228, %[srd], %[sz] offen sc1\n\t"
    "s_waitcnt vmcnt(36)\n\t"
    "v_mfma_f32_16x16x32_bf16 v[16:19], v[172:175], v[212:215], v[16:19]\n\t"
    "s_setprio 0\n\t"
    "v_mfma_f32_16x16x32_bf16 v[20:23], v[172:175], v[216:219], v[20:23]\n\t"
    "s_setprio 1\n\t"
    "v_mfma_f32_16x16x32_bf16 v[24:27], v[172:175], v[220:223], v[24:27]\n\t"
    "v_mfma_f32_16x16x32_bf16 v[28:31], v[172:175], v[224:227], v[28:31]\n\t"
    "buffer_load_dwordx4 v[172:175], v229, %[srd], %[sz] offen sc1\n\t"
    "s_waitcnt vmcnt(35)\n\t"
    "s_setprio 0\n\t"
    "v_mfma_f32_16x16x32_bf16 v[32:35], v[176:179], v[212:215], v[32:35]\n\t"
    "s_setprio 1\n\t"
    "v_mfma_f32_16x16x32_bf16 v[36:39], v[176:179], v[216:219], v[36:39]\n\t"
    "v_mfma_f32_16x16x32_bf16 v[40:43], v[176:179], v[220:223], v[40:43]\n\t"
    "s_setprio 0\n\t"
    "v_mfma_f32_16x16x32_bf16 v[44:47], v[176:179], v[224:227], v[44:47]\n\t"
    "s_setprio 1\n\t"
    "buffer_load_dwordx4 v[176:179], v230, %[srd], %[sz] offen sc1\n\t"
    "s_waitcnt vmcnt(26)\n\t"
    "ds_read_b128 v[196:199], v237 offset:16384\n\t"
    "s_waitcnt vmcnt(34)\n\t"
    "v_mfma_f32_16x16x32_bf16 v[48:51], v[180:183], v[212:215], v[48:51]\n\t"
    "v_mfma_f32_16x16x32_bf16 v[52:55], v[180:183], v[216:219], v[52:55]\n\t"
    "s_setprio 0\n\t"
    "v_mfma_f32_16x16x32_bf16 v[56:59], v[180:183], v[220:223], v[56:59]\n\t"
    "s_setprio 1\n\t"
    "v_mfma_f32_16x16x32_bf16 v[60:63], v[180:183], v[224:227], v[60:63]\n\t"
    "buffer_load_dwordx4 v[180:183], v231, %[srd], %[sz] offen sc1\n\t"
    "ds_read_b128 v[200:203], v237 offset:18432\n\t"
    "s_waitcnt vmcnt(33)\n\t"
    "v_mfma_f32_16x16x32_bf16 v[64:67], v[184:187], v[212:215], v[64:67]\n\t"
    "s_setprio 0\n\t"
    "v_mfma_f32_16x16x32_bf16 v[68:71], v[184:187], v[216:219], v[68:71]\n\t"
    "s_setprio 1\n\t"
    "v_mfma_f32_16x16x32_bf16 v[72:75], v[184:187], v[220:223], v[72:75]\n\t"
    "v_mfma_f32_16x16x32_bf16 v[76:79], v[184:187], v[224:227], v[76:79]\n\t"
    "buffer_load_dwordx4 v[184:187], v232, %[srd], %[sz] offen sc1\n\t"
    "ds_read_b128 v[204:207], v237 offset:20480\n\t"
    "s_waitcnt vmcnt(32)\n\t"
    "s_setprio 0\n\t"
    "v_mfma_f32_16x16x32_bf16 v[80:83], v[188:191], v[212:215], v[80:83]\n\t"
    "s_setprio 1\n\t"
    "v_mfma_f32_16x16x32_bf16 v[84:87], v[188:191], v[216:219], v[84:87]\n\t"
    "v_mfma_f32_16x16x32_bf16 v[88:91], v[188:191], v[220:223], v[88:91]\n\t"
    "s_setprio 0\n\t"
    "v_mfma_f32_16x16x32_bf16 v[92:95], v[188:191], v[224:227], v[92:95]\n\t"
    "s_setprio 1\n\t"
    "buffer_load_dwordx4 v[188:191], v233, %[srd], %[sz] offen sc1\n\t"
    "ds_read_b128 v[208:211], v237 offset:22528\n\t"
    "s_waitcnt vmcnt(31)\n\t"
    "v_mfma_f32_16x16x32_bf16 v[96:99], v[192:195], v[212:215], v[96:99]\n\t"
    "v_mfma_f32_16x16x32_bf16 v[100:103], v[192:195], v[216:219], v[100:103]\n\t"
    "s_setprio 0\n\t"
    "v_mfma_f32_16x16x32_bf16 v[104:107], v[192:195], v[220:223], v[104:107]\n\t"
    "s_setprio 1\n\t"
    "v_mfma_f32_16x16x32_bf16 v[108:111], v[192:195], v[224:227], v[108:111]\n\t"
    "buffer_load_dwordx4 v[192:195], v234, %[srd], %[sz] offen sc1\n\t"
    "s_waitcnt lgkmcnt(0)\n\t"
    "s_waitcnt vmcnt(29)\n\t"
    "v_mfma_f32_16x16x32_bf16 v[0:3], v[112:115], v[196:199], v[0:3]\n\t"
    "s_setprio 0\n\t"
    "v_mfma_f32_16x16x32_bf16 v[4:7], v[112:115], v[200:203], v[4:7]\n\t"
    "s_setprio 1\n\t"
    "v_mfma_f32_16x16x32_bf16 v[8:11], v[112:115], v[204:207], v[8:11]\n\t"
    "v_mfma_f32_16x16x32_bf16 v[12:15], v[112:115], v[208:211], v[12:15]\n\t"
    "s_add_u32 %[sz], %[sz], 55296\n\t"
    "buffer_load_dwordx4 v[112:115], v228, %[srd], %[sz] offen sc1\n\t"
    "s_add_u32 %[swd], %[swd], 128\n\t"
    "s_add_u32 m0, %[ring], 8192\n\t"
    "s_add_u32 %[t], %[swd], 0\n\t"
    "buffer_load_dwordx4 v235, %[srw], %[t] offen lds\n\t"
    "s_waitcnt vmcnt(30)\n\t"
    "s_setprio 0\n\t"
    "v_mfma_f32_16x16x32_bf16 v[16:19], v[116:119], v[196:199], v[16:19]\n\t"
    "s_setprio 1\n\t"
    "v_mfma_f32_16x16x32_bf16 v[20:23], v[116:119], v[200:203], v[20:23]\n\t"
    "v_mfma_f32_16x16x32_bf16 v[24:27], v[116:119], v[204:207], v[24:27]\n\t"
    "s_setprio 0\n\t"
    "v_mfma_f32_16x16x32_bf16 v[28:31], v[116:119], v[208:211], v[28:31]\n\t"
    "s_setprio 1\n\t"
    "buffer_load_dwordx4 v[116:119], v229, %[srd], %[sz] offen sc1\n\t"
    "s_add_u32 m0, %[ring], 9216\n\t"
    "s_add_u32 %[t], %[swd], 32768\n\t"
    "buffer_load_dwordx4 v236, %[srw], %[t] offen lds\n\t"
    "s_waitcnt vmcnt(31)\n\t"
    "v_mfma_f32_16x16x32_bf16 v[32:35], v[120:123], v[196:199], v[32:35]\n\t"
    "v_mfma_f32_16x16x32_bf16 v[36:39], v[120:123], v[200:203], v[36:39]\n\t"
    "s_setprio 0\n\t"
    "v_mfma_f32_16x16x32_bf16 v[40:43], v[120:123], v[204:207], v[40:43]\n\t"
    "s_setprio 1\n\t"
    "v_mfma_f32_16x16x32_bf16 v[44:47], v[120:123], v[208:211], v[44:47]\n\t"
    "buffer_load_dwordx4 v[120:123], v230, %[srd], %[sz] offen sc1\n\t"
    "s_add_u32 m0, %[ring], 10240\n\t"
    "s_add_u32 %[t], %[swd], 65536\n\t"
    "buffer_load_dwordx4 v235, %[srw], %[t] offen lds\n\t"
    "ds_read_b128 v[212:215], v238 offset:16384\n\t"
    "s_waitcnt vmcnt(32)\n\t"
    "v_mfma_f32_16x16x32_bf16 v[48:51], v[124:127], v[196:199], v[48:51]\n\t"
    "s_setprio 0\n\t"
    "v_mfma_f32_16x16x32_bf16 v[52:55], v[124:127], v[200:203], v[52:55]\n\t"
    "s_setprio 1\n\t"
    "v_mfma_f32_16x16x32_bf16 v[56:59], v[124:127], v[204:207], v[56:59]\n\t"
    "v_mfma_f32_16x16x32_bf16 v[60:63], v[124:127], v[208:211], v[60:63]\n\t"
    "buffer_load_dwordx4 v[124:127], v231, %[srd], %[sz] offen sc1\n\t"
    "s_add_u32 m0, %[ring], 11264\n\t"
    "s_add_u32 %[t], %[swd], 98304\n\t"
    "buffer_load_dwordx4 v236, %[srw], %[t] offen lds\n\t"
    "ds_read_b128 v[216:219], v238 offset:18432\n\t"
    "s_waitcnt vmcnt(33)\n\t"
    "s_setprio 0\n\t"
    "v_mfma_f32_16x16x32_bf16 v[64:67], v[128:131], v[196:199], v[64:67]\n\t"
    "s_setprio 1\n\t"
    "v_mfma_f32_16x16x32_bf16 v[68:71], v[128:131], v[200:203], v[68:71]\n\t"
    "v_mfma_f32_16x16x32_bf16 v[72:75], v[128:131], v[204:207], v[72:75]\n\t"
    "s_setprio 0\n\t"
    "v_mfma_f32_16x16x32_bf16 v[76:79], v[128:131], v[208:211], v[76:79]\n\t"
    "s_setprio 1\n\t"
    "buffer_load_dwordx4 v[128:131], v232, %[srd], %[sz] offen sc1\n\t"
    "s_add_u32 m0, %[ring], 12288\n\t"
    "s_add_u32 %[t], %[swd], 131072\n\t"
    "buffer_load_dwordx4 v235, %[srw], %[t] offen lds\n\t"
    "ds_read_b128 v[220:223], v238 offset:20480\n\t"
    "s_waitcnt vmcnt(34)\n\t"
    "v_mfma_f32_16x16x32_bf16 v[80:83], v[132:135], v[196:199], v[80:83]\n\t"
    "v_mfma_f32_16x16x32_bf16 v[84:87], v[132:135], v[200:203], v[84:87]\n\t"
    "s_setprio 0\n\t"
    "v_mfma_f32_16x16x32_bf16 v[88:91], v[132:135], v[204:207], v[88:91]\n\t"
    "s_setprio 1\n\t"
    "v_mfma_f32_16x16x32_bf16 v[92:95], v[132:135], v[208:211], v[92:95]\n\t"
    "buffer_load_dwordx4 v[132:135], v233, %[srd], %[sz] offen sc1\n\t"
    "s_add_u32 m0, %[ring], 13312\n\t"
    "s_add_u32 %[t], %[swd], 163840\n\t"
    "buffer_load_dwordx4 v236, %[srw], %[t] offen lds\n\t"
    "ds_read_b128 v[224:227], v238 offset:22528\n\t"
    "s_waitcnt vmcnt(35)\n\t"
    "v_mfma_f32_16x16x32_bf16 v[96:99], v[136:139], v[196:199], v[96:99]\n\t"
    "s_setprio 0\n\t"
    "v_mfma_f32_16x16x32_bf16 v[100:103], v[136:139], v[200:203], v[100:103]\n\t"
    "s_setprio 1\n\t"
    "v_mfma_f32_16x16x32_bf16 v[104:107], v[136:139], v[204:207], v[104:107]\n\t"
    "v_mfma_f32_16x16x32_bf16 v[108:111], v[136:139], v[208:211], v[108:111]\n\t"
    "buffer_load_dwordx4 v[136:139], v234, %[srd], %[sz] offen sc1\n\t"
    "s_add_u32 m0, %[ring], 14336\n\t"
    "s_add_u32 %[t], %[swd], 196608\n\t"
    "buffer_load_dwordx4 v235, %[srw], %[t] offen lds\n\t"
    "s_add_u32 m0, %[ring], 15360\n\t"
    "s_add_u32 %[t], %[swd], 229376\n\t"
    "buffer_load_dwordx4 v236, %[srw], %[t] offen lds\n\t"
    "s_waitcnt lgkmcnt(0)\n\t"
    "s_waitcnt vmcnt(37)\n\t"
    "s_setprio 0\n\t"
    "v_mfma_f32_16x16x32_bf16 v[0:3], v[140:143], v[212:215], v[0:3]\n\t"
    "s_setprio 1\n\t"
    "v_mfma_f32_16x16x32_bf16 v[4:7], v[140:143], v[216:219], v[4:7]\n\t"
    "v_mfma_f32_16x16x32_bf16 v[8:11], v[140:143], v[220:223], v[8:11]\n\t"
    "s_setprio 0\n\t"
    "v_mfma_f32_16x16x32_bf16 v[12:15], v[140:143], v[224:227], v[12:15]\n\t"
    "s_setprio 1\n\t"
    "s_add_u32 %[sz], %[sz], 55296\n\t"
    "buffer_load_dwordx4 v[140:143], v228, %[srd], %[sz] offen sc1\n\t"
    "s_waitcnt vmcnt(36)\n\t"
    "v_mfma_f32_16x16x32_bf16 v[16:19], v[144:147], v[212:215], v[16:19]\n\t"
    "v_mfma_f32_16x16x32_bf16 v[20:23], v[144:147], v[216:219], v[20:23]\n\t"
    "s_setprio 0\n\t"
    "v_mfma_f32_16x16x32_bf16 v[24:27], v[144:147], v[220:223], v[24:27]\n\t"
    "s_setprio 1\n\t"
    "v_mfma_f32_16x16x32_bf16 v[28:31], v[144:147], v[224:227], v[28:31]\n\t"
    "buffer_load_dwordx4 v[144:147], v229, %[srd], %[sz] offen sc1\n\t"
    "s_waitcnt vmcnt(35)\n\t"
    "v_mfma_f32_16x16x32_bf16 v[32:35], v[148:151], v[212:215], v[32:35]\n\t"
    "s_setprio 0\n\t"
    "v_mfma_f32_16x16x32_bf16 v[36:39], v[148:151], v[216:219], v[36:39]\n\t"
    "s_setprio 1\n\t"
    "v_mfma_f32_16x16x32_bf16 v[40:43], v[148:151], v[220:223], v[40:43]\n\t"
    "v_mfma_f32_16x16x32_bf16 v[44:47], v[148:151], v[224:227], v[44:47]\n\t"
    "buffer_load_dwordx4 v[148:151], v230, %[srd], %[sz] offen sc1\n\t"
    "s_waitcnt vmcnt(26)\n\t"
    "ds_read_b128 v[196:199], v237 offset:0\n\t"
    "s_waitcnt vmcnt(34)\n\t"
    "s_setprio 0\n\t"
    "v_mfma_f32_16x16x32_bf16 v[48:51], v[152:155], v[212:215], v[48:51]\n\t"
    "s_setprio 1\n\t"
    "v_mfma_f32_16x16x32_bf16 v[52:55], v[152:155], v[216:219], v[52:55]\n\t"
    "v_mfma_f32_16x16x32_bf16 v[56:59], v[152:155], v[220:223], v[56:59]\n\t"
    "s_setprio 0\n\t"
    "v_mfma_f32_16x16x32_bf16 v[60:63], v[152:155], v[224:227], v[60:63]\n\t"
    "s_setprio 1\n\t"
    "buffer_load_dwordx4 v[152:155], v231, %[srd], %[sz] offen sc1\n\t"
    "ds_read_b128 v[200:203], v237 offset:2048\n\t"
    "s_waitcnt vmcnt(33)\n\t"
    "v_mfma_f32_16x16x32_bf16 v[64:67], v[156:159], v[212:215], v[64:67]\n\t"
    "v_mfma_f32_16x16x32_bf16 v[68:71], v[156:159], v[216:219], v[68:71]\n\t"
    "s_setprio 0\n\t"
    "v_mfma_f32_16x16x32_bf16 v[72:75], v[156:159], v[220:223], v[72:75]\n\t"
    "s_setprio 1\n\t"
    "v_mfma_f32_16x16x32_bf16 v[76:79], v[156:159], v[224:227], v[76:79]\n\t"
    "buffer_load_dwordx4 v[156:159], v232, %[srd], %[sz] offen sc1\n\t"
    "ds_read_b128 v[204:207], v237 offset:4096\n\t"
    "s_waitcnt vmcnt(32)\n\t"
    "v_mfma_f32_16x16x32_bf16 v[80:83], v[160:163], v[212:215], v[80:83]\n\t"
    "s_setprio 0\n\t"
    "v_mfma_f32_16x16x32_bf16 v[84:87], v[160:163], v[216:219], v[84:87]\n\t"
    "s_setprio 1\n\t"
    "v_mfma_f32_16x16x32_bf16 v[88:91], v[160:163], v[220:223], v[88:91]\n\t"
    "v_mfma_f32_16x16x32_bf16 v[92:95], v[160:163], v[224:227], v[92:95]\n\t"
    "buffer_load_dwordx4 v[160:163], v233, %[srd], %[sz] offen sc1\n\t"
    "ds_read_b128 v[208:211], v237 offset:6144\n\t"
    "s_waitcnt vmcnt(31)\n\t"
    "s_setprio 0\n\t"
    "v_mfma_f32_16x16x32_bf16 v[96:99], v[164:167], v[212:215], v[96:99]\n\t"
    "s_setprio 1\n\t"
    "v_mfma_f32_16x16x32_bf16 v[100:103], v[164:167], v[216:219], v[100:103]\n\t"
    "v_mfma_f32_16x16x32_bf16 v[104:107], v[164:167], v[220:223], v[104:107]\n\t"
    "s_setprio 0\n\t"
    "v_mfma_f32_16x16x32_bf16 v[108:111], v[164:167], v[224:227], v[108:111]\n\t"
    "s_setprio 1\n\t"
    "buffer_load_dwordx4 v[164:167], v234, %[srd], %[sz] offen sc1\n\t"
    "s_waitcnt lgkmcnt(0)\n\t"
    "s_waitcnt vmcnt(28)\n\t"
    "v_mfma_f32_16x16x32_bf16 v[0:3], v[168:171], v[196:199], v[0:3]\n\t"
    "v_mfma_f32_16x16x32_bf16 v[4:7], v[168:171], v[200:203], v[4:7]\n\t"
    "s_setprio 0\n\t"
    "v_mfma_f32_16x16x32_bf16 v[8:11], v[168:171], v[204:207], v[8:11]\n\t"
    "s_setprio 1\n\t"
    "v_mfma_f32_16x16x32_bf16 v[12:15], v[168:171], v[208:211], v[12:15]\n\t"
    "s_add_u32 %[sz], %[sz], 55296\n\t"
    "buffer_load_dwordx4 v[168:171], v228, %[srd], %[sz] offen sc1\n\t"
    "s_add_u32 %[swd], %[swd], 128\n\t"
    "s_add_u32 m0, %[ring], 16384\n\t"
    "s_add_u32 %[t], %[swd], 0\n\t"
    "buffer_load_dwordx4 v235, %[srw], %[t] offen lds\n\t"
    "s_waitcnt vmcnt(29)\n\t"
    "v_mfma_f32_16x16x32_bf16 v[16:19], v[172:175], v[196:199], v[16:19]\n\t"
    "s_setprio 0\n\t"
    "v_mfma_f32_16x16x32_bf16 v[20:23], v[172:175], v[200:203], v[20:23]\n\t"
    "s_setprio 1\n\t"
    "v_mfma_f32_16x16x32_bf16 v[24:27], v[172:175], v[204:207], v[24:27]\n\t"
    "v_mfma_f32_16x16x32_bf16 v[28:31], v[172:175], v[208:211], v[28:31]\n\t"
    "buffer_load_dwordx4 v[172:175], v229, %[srd], %[sz] offen sc1\n\t"
    "s_add_u32 m0, %[ring], 17408\n\t"
    "s_add_u32 %[t], %[swd], 32768\n\t"
    "buffer_load_dwordx4 v236, %[srw], %[t] offen lds\n\t"
    "s_waitcnt vmcnt(30)\n\t"
    "s_setprio 0\n\t"
    "v_mfma_f32_16x16x32_bf16 v[32:35], v[176:179], v[196:199], v[32:35]\n\t"
    "s_setprio 1\n\t"
    "v_mfma_f32_16x16x32_bf16 v[36:39], v[176:179], v[200:203], v[36:39]\n\t"
    "v_mfma_f32_16x16x32_bf16 v[40:43], v[176:179], v[204:207], v[40:43]\n\t"
    "s_setprio 0\n\t"
    "v_mfma_f32_16x16x32_bf16 v[44:47], v[176:179], v[208:211], v[44:47]\n\t"
    "s_setprio 1\n\t"
    "buffer_load_dwordx4 v[176:179], v230, %[srd], %[sz] offen sc1\n\t"
    "s_add_u32 m0, %[ring], 18432\n\t"
    "s_add_u32 %[t], %[swd], 65536\n\t"
    "buffer_load_dwordx4 v235, %[srw], %[t] offen lds\n\t"
    "ds_read_b128 v[212:215], v238 offset:0\n\t"
    "s_waitcnt vmcnt(31)\n\t"
    "v_mfma_f32_16x16x32_bf16 v[48:51], v[180:183], v[196:199], v[48:51]\n\t"
    "v_mfma_f32_16x16x32_bf16 v[52:55], v[180:183], v[200:203], v[52:55]\n\t"
    "s_setprio 0\n\t"
    "v_mfma_f32_16x16x32_bf16 v[56:59], v[180:183], v[204:207], v[56:59]\n\t"
    "s_setprio 1\n\t"
    "v_mfma_f32_16x16x32_bf16 v[60:63], v[180:183], v[208:211], v[60:63]\n\t"
    "buffer_load_dwordx4 v[180:183], v231, %[srd], %[sz] offen sc1\n\t"
    "s_add_u32 m0, %[ring], 19456\n\t"
    "s_add_u32 %[t], %[swd], 98304\n\t"
    "buffer_load_dwordx4 v236, %[srw], %[t] offen lds\n\t"
    "ds_read_b128 v[216:219], v238 offset:2048\n\t"
    "s_waitcnt vmcnt(32)\n\t"
    "v_mfma_f32_16x16x32_bf16 v[64:67], v[184:187], v[196:199], v[64:67]\n\t"
    "s_setprio 0\n\t"
    "v_mfma_f32_16x16x32_bf16 v[68:71], v[184:187], v[200:203], v[68:71]\n\t"
    "s_setprio 1\n\t"
    "v_mfma_f32_16x16x32_bf16 v[72:75], v[184:187], v[204:207], v[72:75]\n\t"
    "v_mfma_f32_16x16x32_bf16 v[76:79], v[184:187], v[208:211], v[76:79]\n\t"
    "buffer_load_dwordx4 v[184:187], v232, %[srd], %[sz] offen sc1\n\t"
    "s_add_u32 m0, %[ring], 20480\n\t"
    "s_add_u32 %[t], %[swd], 131072\n\t"
    "buffer_load_dwordx4 v235, %[srw], %[t] offen lds\n\t"
    "ds_read_b128 v[220:223], v238 offset:4096\n\t"
    "s_waitcnt vmcnt(33)\n\t"
    "s_setprio 0\n\t"
    "v_mfma_f32_16x16x32_bf16 v[80:83], v[188:191], v[196:199], v[80:83]\n\t"
    "s_setprio 1\n\t"
    "v_mfma_f32_16x16x32_bf16 v[84:87], v[188:191], v[200:203], v[84:87]\n\t"
    "v_mfma_f32_16x16x32_bf16 v[88:91], v[188:191], v[204:207], v[88:91]\n\t"
    "s_setprio 0\n\t"
    "v_mfma_f32_16x16x32_bf16 v[92:95], v[188:191], v[208:211], v[92:95]\n\t"
    "s_setprio 1\n\t"
    "buffer_load_dwordx4 v[188:191], v233, %[srd], %[sz] offen sc1\n\t"
    "s_add_u32 m0, %[ring], 21504\n\t"
    "s_add_u32 %[t], %[swd], 163840\n\t"
    "buffer_load_dwordx4 v236, %[srw], %[t] offen lds\n\t"
    "ds_read_b128 v[224:227], v238 offset:6144\n\t"
    "s_waitcnt vmcnt(34)\n\t"
    "v_mfma_f32_16x16x32_bf16 v[96:99], v[192:195], v[196:199], v[96:99]\n\t"
    "v_mfma_f32_16x16x32_bf16 v[100:103], v[192:195], v[200:203], v[100:103]\n\t"
    "s_setprio 0\n\t"
    "v_mfma_f32_16x16x32_bf16 v[104:107], v[192:195], v[204:207], v[104:107]\n\t"
    "s_setprio 1\n\t"
    "v_mfma_f32_16x16x32_bf16 v[108:111], v[192:195], v[208:211], v[108:111]\n\t"
    "buffer_load_dwordx4 v[192:195], v234, %[srd], %[sz] offen sc1\n\t"
    "s_add_u32 m0, %[ring], 22528\n\t"
    "s_add_u32 %[t], %[swd], 196608\n\t"
    "buffer_load_dwordx4 v235, %[srw], %[t] offen lds\n\t"
    "s_add_u32 m0, %[ring], 23552\n\t"
    "s_add_u32 %[t], %[swd], 229376\n\t"
    "buffer_load_dwordx4 v236, %[srw], %[t] offen lds\n\t"
    "s_waitcnt lgkmcnt(0)\n\t"
    "s_waitcnt vmcnt(36)\n\t"
    "v_mfma_f32_16x16x32_bf16 v[0:3], v[112:115], v[212:215], v[0:3]\n\t"
    "s_setprio 0\n\t"
    "v_mfma_f32_16x16x32_bf16 v[4:7], v[112:115], v[216:219], v[4:7]\n\t"
    "s_setprio 1\n\t"
    "v_mfma_f32_16x16x32_bf16 v[8:11], v[112:115], v[220:223], v[8:11]\n\t"
    "v_mfma_f32_16x16x32_bf16 v[12:15], v[112:115], v[224:227], v[12:15]\n\t"
    "s_add_u32 %[sz], %[sz], 55296\n\t"
    "buffer_load_dwordx4 v[112:115], v228, %[srd], %[sz] offen sc1\n\t"
    "s_waitcnt vmcnt(35)\n\t"
    "s_setprio 0\n\t"
    "v_mfma_f32_16x16x32_bf16 v[16:19], v[116:119], v[212:215], v[16:19]\n\t"
    "s_setprio 1\n\t"
    "v_mfma_f32_16x16x32_bf16 v[20:23], v[116:119], v[216:219], v[20:23]\n\t"
    "v_mfma_f32_16x16x32_bf16 v[24:27], v[116:119], v[220:223], v[24:27]\n\t"
    "s_setprio 0\n\t"
    "v_mfma_f32_16x16x32_bf16 v[28:31], v[116:119], v[224:227], v[28:31]\n\t"
    "s_setprio 1\n\t"
    "buffer_load_dwordx4 v[116:119], v229, %[srd], %[sz] offen sc1\n\t"
    "s_waitcnt vmcnt(34)\n\t"
    "v_mfma_f32_16x16x32_bf16 v[32:35], v[120:123], v[212:215], v[32:35]\n\t"
    "v_mfma_f32_16x16x32_bf16 v[36:39], v[120:123], v[216:219], v[36:39]\n\t"
    "s_setprio 0\n\t"
    "v_mfma_f32_16x16x32_bf16 v[40:43], v[120:123], v[220:223], v[40:43]\n\t"
    "s_setprio 1\n\t"
    "v_mfma_f32_16x16x32_bf16 v[44:47], v[120:123], v[224:227], v[44:47]\n\t"
    "buffer_load_dwordx4 v[120:123], v230, %[srd], %[sz] offen sc1\n\t"
    "s_waitcnt vmcnt(25)\n\t"
    "ds_read_b128 v[196:199], v237 offset:8192\n\t"
    "s_waitcnt vmcnt(33)\n\t"
    "v_mfma_f32_16x16x32_bf16 v[48:51], v[124:127], v[212:215], v[48:51]\n\t"
    "s_setprio 0\n\t"
    "v_mfma_f32_16x16x32_bf16 v[52:55], v[124:127], v[216:219], v[52:55]\n\t"
    "s_setprio 1\n\t"
    "v_mfma_f32_16x16x32_bf16 v[56:59], v[124:127], v[220:223], v[56:59]\n\t"
    "v_mfma_f32_16x16x32_bf16 v[60:63], v[124:127], v[224:227], v[60:63]\n\t"
    "buffer_load_dwordx4 v[124:127], v231, %[srd], %[sz] offen sc1\n\t"
    "ds_read_b128 v[200:203], v237 offset:10240\n\t"
    "s_waitcnt vmcnt(32)\n\t"
    "s_setprio 0\n\t"
    "v_mfma_f32_16x16x32_bf16 v[64:67], v[128:131], v[212:215], v[64:67]\n\t"
    "s_setprio 1\n\t"
    "v_mfma_f32_16x16x32_bf16 v[68:71], v[128:131], v[216:219], v[68:71]\n\t"
    "v_mfma_f32_16x16x32_bf16 v[72:75], v[128:131], v[220:223], v[72:75]\n\t"
    "s_setprio 0\n\t"
    "v_mfma_f32_16x16x32_bf16 v[76:79], v[128:131], v[224:227], v[76:79]\n\t"
    "s_setprio 1\n\t"
    "buffer_load_dwordx4 v[128:131], v232, %[srd], %[sz] offen sc1\n\t"
    "ds_read_b128 v[204:207], v237 offset:12288\n\t"
    "s_waitcnt vmcnt(31)\n\t"
    "v_mfma_f32_16x16x32_bf16 v[80:83], v[132:135], v[212:215], v[80:83]\n\t"
    "v_mfma_f32_16x16x32_bf16 v[84:87], v[132:135], v[216:219], v[84:87]\n\t"
    "s_setprio 0\n\t"
    "v_mfma_f32_16x16x32_bf16 v[88:91], v[132:135], v[220:223], v[88:91]\n\t"
    "s_setprio 1\n\t"
    "v_mfma_f32_16x16x32_bf16 v[92:95], v[132:135], v[224:227], v[92:95]\n\t"
    "buffer_load_dwordx4 v[132:135], v233, %[srd], %[sz] offen sc1\n\t"
    "ds_read_b128 v[208:211], v237 offset:14336\n\t"
    "s_waitcnt vmcnt(30)\n\t"
    "v_mfma_f32_16x16x32_bf16 v[96:99], v[136:139], v[212:215], v[96:99]\n\t"
    "s_setprio 0\n\t"
    "v_mfma_f32_16x16x32_bf16 v[100:103], v[136:139], v[216:219], v[100:103]\n\t"
    "s_setprio 1\n\t"
    "v_mfma_f32_16x16x32_bf16 v[104:107], v[136:139], v[220:223], v[104:107]\n\t"
    "v_mfma_f32_16x16x32_bf16 v[108:111], v[136:139], v[224:227], v[108:111]\n\t"
    "buffer_load_dwordx4 v[136:139], v234, %[srd], %[sz] offen sc1\n\t"
    "s_waitcnt lgkmcnt(0)\n\t"
    "s_waitcnt vmcnt(28)\n\t"
    "s_setprio 0\n\t"
    "v_mfma_f32_16x16x32_bf16 v[0:3], v[140:143], v[196:199], v[0:3]\n\t"
    "s_setprio 1\n\t"
    "v_mfma_f32_16x16x32_bf16 v[4:7], v[140:143], v[200:203], v[4:7]\n\t"
    "v_mfma_f32_16x16x32_bf16 v[8:11], v[140:143], v[204:207], v[8:11]\n\t"
    "s_setprio 0\n\t"
    "v_mfma_f32_16x16x32_bf16 v[12:15], v[140:143], v[208:211], v[12:15]\n\t"
    "s_setprio 1\n\t"
    "s_add_u32 %[sz], %[sz], 55296\n\t"
    "buffer_load_dwordx4 v[140:143], v228, %[srd], %[sz] offen sc1\n\t"
    "s_add_u32 %[swd], %[swd], 128\n\t"
    "s_add_u32 m0, %[ring], 0\n\t"
    "s_add_u32 %[t], %[swd], 0\n\t"
    "buffer_load_dwordx4 v235, %[srw], %[t] offen lds\n\t"
    "s_waitcnt vmcnt(29)\n\t"
    "v_mfma_f32_16x16x32_bf16 v[16:19], v[144:147], v[196:199], v[16:19]\n\t"
    "v_mfma_f32_16x16x32_bf16 v[20:23], v[144:147], v[200:203], v[20:23]\n\t"
    "s_setprio 0\n\t"
    "v_mfma_f32_16x16x32_bf16 v[24:27], v[144:147], v[204:207], v[24:27]\n\t"
    "s_setprio 1\n\t"
    "v_mfma_f32_16x16x32_bf16 v[28:31], v[144:147], v[208:211], v[28:31]\n\t"
    "buffer_load_dwordx4 v[144:147], v229, %[srd], %[sz] offen sc1\n\t"
    "s_add_u32 m0, %[ring], 1024\n\t"
    "s_add_u32 %[t], %[swd], 32768\n\t"
    "buffer_load_dwordx4 v236, %[srw], %[t] offen lds\n\t"
    "s_waitcnt vmcnt(30)\n\t"
    "v_mfma_f32_16x16x32_bf16 v[32:35], v[148:151], v[196:199], v[32:35]\n\t"
    "s_setprio 0\n\t"
    "v_mfma_f32_16x16x32_bf16 v[36:39], v[148:151], v[200:203], v[36:39]\n\t"
    "s_setprio 1\n\t"
    "v_mfma_f32_16x16x32_bf16 v[40:43], v[148:151], v[204:207], v[40:43]\n\t"
    "v_mfma_f32_16x16x32_bf16 v[44:47], v[148:151], v[208:211], v[44:47]\n\t"
    "buffer_load_dwordx4 v[148:151], v230, %[srd], %[sz] offen sc1\n\t"
    "s_add_u32 m0, %[ring], 2048\n\t"
    "s_add_u32 %[t], %[swd], 65536\n\t"
    "buffer_load_dwordx4 v235, %[srw], %[t] offen lds\n\t"
    "ds_read_b128 v[212:215], v238 offset:8192\n\t"
    "s_waitcnt vmcnt(31)\n\t"
    "s_setprio 0\n\t"
    "v_mfma_f32_16x16x32_bf16 v[48:51], v[152:155], v[196:199], v[48:51]\n\t"
    "s_setprio 1\n\t"
    "v_mfma_f32_16x16x32_bf16 v[52:55], v[152:155], v[200:203], v[52:55]\n\t"
    "v_mfma_f32_16x16x32_bf16 v[56:59], v[152:155], v[204:207], v[56:59]\n\t"
    "s_setprio 0\n\t"
    "v_mfma_f32_16x16x32_bf16 v[60:63], v[152:155], v[208:211], v[60:63]\n\t"
    "s_setprio 1\n\t"
    "buffer_load_dwordx4 v[152:155], v231, %[srd], %[sz] offen sc1\n\t"
    "s_add_u32 m0, %[ring], 3072\n\t"
    "s_add_u32 %[t], %[swd], 98304\n\t"
    "buffer_load_dwordx4 v236, %[srw], %[t] offen lds\n\t"
    "ds_read_b128 v[216:219], v238 offset:10240\n\t"
    "s_waitcnt vmcnt(32)\n\t"
    "v_mfma_f32_16x16x32_bf16 v[64:67], v[156:159], v[196:199], v[64:67]\n\t"
    "v_mfma_f32_16x16x32_bf16 v[68:71], v[156:159], v[200:203], v[68:71]\n\t"
    "s_setprio 0\n\t"
    "v_mfma_f32_16x16x32_bf16 v[72:75], v[156:159], v[204:207], v[72:75]\n\t"
    "s_setprio 1\n\t"
    "v_mfma_f32_16x16x32_bf16 v[76:79], v[156:159], v[208:211], v[76:79]\n\t"
    "buffer_load_dwordx4 v[156:159], v232, %[srd], %[sz] offen sc1\n\t"
    "s_add_u32 m0, %[ring], 4096\n\t"
    "s_add_u32 %[t], %[swd], 131072\n\t"
    "buffer_load_dwordx4 v235, %[srw], %[t] offen lds\n\t"
    "ds_read_b128 v[220:223], v238 offset:12288\n\t"
    "s_waitcnt vmcnt(33)\n\t"
    "v_mfma_f32_16x16x32_bf16 v[80:83], v[160:163], v[196:199], v[80:83]\n\t"
    "s_setprio 0\n\t"
    "v_mfma_f32_16x16x32_bf16 v[84:87], v[160:163], v[200:203], v[84:87]\n\t"
    "s_setprio 1\n\t"
    "v_mfma_f32_16x16x32_bf16 v[88:91], v[160:163], v[204:207], v[88:91]\n\t"
    "v_mfma_f32_16x16x32_bf16 v[92:95], v[160:163], v[208:211], v[92:95]\n\t"
    "buffer_load_dwordx4 v[160:163], v233, %[srd], %[sz] offen sc1\n\t"
    "s_add_u32 m0, %[ring], 5120\n\t"
    "s_add_u32 %[t], %[swd], 163840\n\t"
    "buffer_load_dwordx4 v236, %[srw], %[t] offen lds\n\t"
    "ds_read_b128 v[224:227], v238 offset:14336\n\t"
    "s_waitcnt vmcnt(34)\n\t"
    "s_setprio 0\n\t"
    "v_mfma_f32_16x16x32_bf16 v[96:99], v[164:167], v[196:199], v[96:99]\n\t"
    "s_setprio 1\n\t"
    "v_mfma_f32_16x16x32_bf16 v[100:103], v[164:167], v[200:203], v[100:103]\n\t"
    "v_mfma_f32_16x16x32_bf16 v[104:107], v[164:167], v[204:207], v[104:107]\n\t"
    "s_setprio 0\n\t"
    "v_mfma_f32_16x16x32_bf16 v[108:111], v[164:167], v[208:211], v[108:111]\n\t"
    "s_setprio 1\n\t"
    "buffer_load_dwordx4 v[164:167], v234, %[srd], %[sz] offen sc1\n\t"
    "s_add_u32 m0, %[ring], 6144\n\t"
    "s_add_u32 %[t], %[swd], 196608\n\t"
    "buffer_load_dwordx4 v235, %[srw], %[t] offen lds\n\t"
    "s_add_u32 m0, %[ring], 7168\n\t"
    "s_add_u32 %[t], %[swd], 229376\n\t"
    "buffer_load_dwordx4 v236, %[srw], %[t] offen lds\n\t"
    "s_waitcnt lgkmcnt(0)\n\t"
    "s_waitcnt vmcnt(36)\n\t"
    "v_mfma_f32_16x16x32_bf16 v[0:3], v[168:171], v[212:215], v[0:3]\n\t"
    "v_mfma_f32_16x16x32_bf16 v[4:7], v[168:171], v[216:219], v[4:7]\n\t"
    "s_setprio 0\n\t"
    "v_mfma_f32_16x16x32_bf16 v[8:11], v[168:171], v[220:223], v[8:11]\n\t"
    "s_setprio 1\n\t"
    "v_mfma_f32_16x16x32_bf16 v[12:15], v[168:171], v[224:227], v[12:15]\n\t"
    "s_add_u32 %[sz], %[sz], 55296\n\t"
    "buffer_load_dwordx4 v[168:171], v228, %[srd], %[sz] offen sc1\n\t"
    "s_waitcnt vmcnt(35)\n\t"
    "v_mfma_f32_16x16x32_bf16 v[16:19], v[172:175], v[212:215], v[16:19]\n\t"
    "s_setprio 0\n\t"
    "v_mfma_f32_16x16x32_bf16 v[20:23], v[172:175], v[216:219], v[20:23]\n\t"
    "s_setprio 1\n\t"
    "v_mfma_f32_16x16x32_bf16 v[24:27], v[172:175], v[220:223], v[24:27]\n\t"
    "v_mfma_f32_16x16x32_bf16 v[28:31], v[172:175], v[224:227], v[28:31]\n\t"
    "buffer_load_dwordx4 v[172:175], v229, %[srd], %[sz] offen sc1\n\t"
    "s_waitcnt vmcnt(34)\n\t"
    "s_setprio 0\n\t"
    "v_mfma_f32_16x16x32_bf16 v[32:35], v[176:179], v[212:215], v[32:35]\n\t"
    "s_setprio 1\n\t"
    "v_mfma_f32_16x16x32_bf16 v[36:39], v[176:179], v[216:219], v[36:39]\n\t"
    "v_mfma_f32_16x16x32_bf16 v[40:43], v[176:179], v[220:223], v[40:43]\n\t"
    "s_setprio 0\n\t"
    "v_mfma_f32_16x16x32_bf16 v[44:47], v[176:179], v[224:227], v[44:47]\n\t"
    "s_setprio 1\n\t"
    "buffer_load_dwordx4 v[176:179], v230, %[srd], %[sz] offen sc1\n\t"
    "s_waitcnt vmcnt(25)\n\t"
    "ds_read_b128 v[196:199], v237 offset:16384\n\t"
    "s_waitcnt vmcnt(33)\n\t"
    "v_mfma_f32_16x16x32_bf16 v[48:51], v[180:183], v[212:215], v[48:51]\n\t"
    "v_mfma_f32_16x16x32_bf16 v[52:55], v[180:183], v[216:219], v[52:55]\n\t"
    "s_setprio 0\n\t"
    "v_mfma_f32_16x16x32_bf16 v[56:59], v[180:183], v[220:223], v[56:59]\n\t"
    "s_setprio 1\n\t"
    "v_mfma_f32_16x16x32_bf16 v[60:63], v[180:183], v[224:227], v[60:63]\n\t"
    "buffer_load_dwordx4 v[180:183], v231, %[srd], %[sz] offen sc1\n\t"
    "ds_read_b128 v[200:203], v237 offset:18432\n\t"
    "s_waitcnt vmcnt(32)\n\t"
    "v_mfma_f32_16x16x32_bf16 v[64:67], v[184:187], v[212:215], v[64:67]\n\t"
    "s_setprio 0\n\t"
    "v_mfma_f32_16x16x32_bf16 v[68:71], v[184:187], v[216:219], v[68:71]\n\t"
    "s_setprio 1\n\t"
    "v_mfma_f32_16x16x32_bf16 v[72:75], v[184:187], v[220:223], v[72:75]\n\t"
    "v_mfma_f32_16x16x32_bf16 v[76:79], v[184:187], v[224:227], v[76:79]\n\t"
    "buffer_load_dwordx4 v[184:187], v232, %[srd], %[sz] offen sc1\n\t"
    "ds_read_b128 v[204:207], v237 offset:20480\n\t"
    "s_waitcnt vmcnt(31)\n\t"
    "s_setprio 0\n\t"
    "v_mfma_f32_16x16x32_bf16 v[80:83], v[188:191], v[212:215], v[80:83]\n\t"
    "s_setprio 1\n\t"
    "v_mfma_f32_16x16x32_bf16 v[84:87], v[188:191], v[216:219], v[84:87]\n\t"
    "v_mfma_f32_16x16x32_bf16 v[88:91], v[188:191], v[220:223], v[88:91]\n\t"
    "s_setprio 0\n\t"
    "v_mfma_f32_16x16x32_bf16 v[92:95], v[188:191], v[224:227], v[92:95]\n\t"
    "s_setprio 1\n\t"
    "buffer_load_dwordx4 v[188:191], v233, %[srd], %[sz] offen sc1\n\t"
    "ds_read_b128 v[208:211], v237 offset:22528\n\t"
    "s_waitcnt vmcnt(30)\n\t"
    "v_mfma_f32_16x16x32_bf16 v[96:99], v[192:195], v[212:215], v[96:99]\n\t"
    "v_mfma_f32_16x16x32_bf16 v[100:103], v[192:195], v[216:219], v[100:103]\n\t"
    "s_setprio 0\n\t"
    "v_mfma_f32_16x16x32_bf16 v[104:107], v[192:195], v[220:223], v[104:107]\n\t"
    "s_setprio 1\n\t"
    "v_mfma_f32_16x16x32_bf16 v[108:111], v[192:195], v[224:227], v[108:111]\n\t"
    "buffer_load_dwordx4 v[192:195], v234, %[srd], %[sz] offen sc1\n\t"
    "s_waitcnt lgkmcnt(0)\n\t"
    "s_waitcnt vmcnt(28)\n\t"
    "buffer_load_dwordx2 v[240:241], v239, %[srd], 0 offen sc1\n\t"
    "v_mfma_f32_16x16x32_bf16 v[0:3], v[112:115], v[196:199], v[0:3]\n\t"
    "s_setprio 0\n\t"
    "v_mfma_f32_16x16x32_bf16 v[4:7], v[112:115], v[200:203], v[4:7]\n\t"
    "s_setprio 1\n\t"
    "v_mfma_f32_16x16x32_bf16 v[8:11], v[112:115], v[204:207], v[8:11]\n\t"
    "v_mfma_f32_16x16x32_bf16 v[12:15], v[112:115], v[208:211], v[12:15]\n\t"
    "s_add_u32 %[sz], %[sz], 55296\n\t"
    "buffer_load_dwordx4 v[112:115], v228, %[srd], %[sz] offen sc1\n\t"
    "s_add_u32 %[swd], %[swd], 128\n\t"
    "s_add_u32 m0, %[ring], 8192\n\t"
    "s_add_u32 %[t], %[swd], 0\n\t"
    "buffer_load_dwordx4 v235, %[srw], %[t] offen lds\n\t"
    "s_waitcnt vmcnt(30)\n\t"
    "s_setprio 0\n\t"
    "v_mfma_f32_16x16x32_bf16 v[16:19], v[116:119], v[196:199], v[16:19]\n\t"
    "s_setprio 1\n\t"
    "v_mfma_f32_16x16x32_bf16 v[20:23], v[116:119], v[200:203], v[20:23]\n\t"
    "v_mfma_f32_16x16x32_bf16 v[24:27], v[116:119], v[204:207], v[24:27]\n\t"
    "s_setprio 0\n\t"
    "v_mfma_f32_16x16x32_bf16 v[28:31], v[116:119], v[208:211], v[28:31]\n\t"
    "s_setprio 1\n\t"
    "buffer_load_dwordx4 v[116:119], v229, %[srd], %[sz] offen sc1\n\t"
    "s_add_u32 m0, %[ring], 9216\n\t"
    "s_add_u32 %[t], %[swd], 32768\n\t"
    "buffer_load_dwordx4 v236, %[srw], %[t] offen lds\n\t"
    "s_waitcnt vmcnt(31)\n\t"
    "v_mfma_f32_16x16x32_bf16 v[32:35], v[120:123], v[196:199], v[32:35]\n\t"
    "v_mfma_f32_16x16x32_bf16 v[36:39], v[120:123], v[200:203], v[36:39]\n\t"
    "s_setprio 0\n\t"
    "v_mfma_f32_16x16x32_bf16 v[40:43], v[120:123], v[204:207], v[40:43]\n\t"
    "s_setprio 1\n\t"
    "v_mfma_f32_16x16x32_bf16 v[44:47], v[120:123], v[208:211], v[44:47]\n\t"
    "buffer_load_dwordx4 v[120:123], v230, %[srd], %[sz] offen sc1\n\t"
    "s_add_u32 m0, %[ring], 10240\n\t"
    "s_add_u32 %[t], %[swd], 65536\n\t"
    "buffer_load_dwordx4 v235, %[srw], %[t] offen lds\n\t"
    "ds_read_b128 v[212:215], v238 offset:16384\n\t"
    "s_waitcnt vmcnt(32)\n\t"
    "v_mfma_f32_16x16x32_bf16 v[48:51], v[124:127], v[196:199], v[48:51]\n\t"
    "s_setprio 0\n\t"
    "v_mfma_f32_16x16x32_bf16 v[52:55], v[124:127], v[200:203], v[52:55]\n\t"
    "s_setprio 1\n\t"
    "v_mfma_f32_16x16x32_bf16 v[56:59], v[124:127], v[204:207], v[56:59]\n\t"
    "v_mfma_f32_16x16x32_bf16 v[60:63], v[124:127], v[208:211], v[60:63]\n\t"
    "buffer_load_dwordx4 v[124:127], v231, %[srd], %[sz] offen sc1\n\t"
    "s_add_u32 m0, %[ring], 11264\n\t"
    "s_add_u32 %[t], %[swd], 98304\n\t"
    "buffer_load_dwordx4 v236, %[srw], %[t] offen lds\n\t"
    "ds_read_b128 v[216:219], v238 offset:18432\n\t"
    "s_waitcnt vmcnt(33)\n\t"
    "s_setprio 0\n\t"
    "v_mfma_f32_16x16x32_bf16 v[64:67], v[128:131], v[196:199], v[64:67]\n\t"
    "s_setprio 1\n\t"
    "v_mfma_f32_16x16x32_bf16 v[68:71], v[128:131], v[200:203], v[68:71]\n\t"
    "v_mfma_f32_16x16x32_bf16 v[72:75], v[128:131], v[204:207], v[72:75]\n\t"
    "s_setprio 0\n\t"
    "v_mfma_f32_16x16x32_bf16 v[76:79], v[128:131], v[208:211], v[76:79]\n\t"
    "s_setprio 1\n\t"
    "buffer_load_dwordx4 v[128:131], v232, %[srd], %[sz] offen sc1\n\t"
    "s_add_u32 m0, %[ring], 12288\n\t"
    "s_add_u32 %[t], %[swd], 131072\n\t"
    "buffer_load_dwordx4 v235, %[srw], %[t] offen lds\n\t"
    "ds_read_b128 v[220:223], v238 offset:20480\n\t"
    "s_waitcnt vmcnt(34)\n\t"
    "v_mfma_f32_16x16x32_bf16 v[80:83], v[132:135], v[196:199], v[80:83]\n\t"
    "v_mfma_f32_16x16x32_bf16 v[84:87], v[132:135], v[200:203], v[84:87]\n\t"
    "s_setprio 0\n\t"
    "v_mfma_f32_16x16x32_bf16 v[88:91], v[132:135], v[204:207], v[88:91]\n\t"
    "s_setprio 1\n\t"
    "v_mfma_f32_16x16x32_bf16 v[92:95], v[132:135], v[208:211], v[92:95]\n\t"
    "buffer_load_dwordx4 v[132:135], v233, %[srd], %[sz] offen sc1\n\t"
    "s_add_u32 m0, %[ring], 13312\n\t"
    "s_add_u32 %[t], %[swd], 163840\n\t"
    "buffer_load_dwordx4 v236, %[srw], %[t] offen lds\n\t"
    "ds_read_b128 v[224:227], v238 offset:22528\n\t"
    "s_waitcnt vmcnt(35)\n\t"
    "v_mfma_f32_16x16x32_bf16 v[96:99], v[136:139], v[196:199], v[96:99]\n\t"
    "s_setprio 0\n\t"
    "v_mfma_f32_16x16x32_bf16 v[100:103], v[136:139], v[200:203], v[100:103]\n\t"
    "s_setprio 1\n\t"
    "v_mfma_f32_16x16x32_bf16 v[104:107], v[136:139], v[204:207], v[104:107]\n\t"
    "v_mfma_f32_16x16x32_bf16 v[108:111], v[136:139], v[208:211], v[108:111]\n\t"
    "buffer_load_dwordx4 v[136:139], v234, %[srd], %[sz] offen sc1\n\t"
    "s_add_u32 m0, %[ring], 14336\n\t"
    "s_add_u32 %[t], %[swd], 196608\n\t"
    "buffer_load_dwordx4 v235, %[srw], %[t] offen lds\n\t"
    "s_add_u32 m0, %[ring], 15360\n\t"
    "s_add_u32 %[t], %[swd], 229376\n\t"
    "buffer_load_dwordx4 v236, %[srw], %[t] offen lds\n\t"
    "s_waitcnt lgkmcnt(0)\n\t"
    "s_waitcnt vmcnt(37)\n\t"
    "s_setprio 0\n\t"
    "v_mfma_f32_16x16x32_bf16 v[0:3], v[140:143], v[212:215], v[0:3]\n\t"
    "s_setprio 1\n\t"
    "v_mfma_f32_16x16x32_bf16 v[4:7], v[140:143], v[216:219], v[4:7]\n\t"
    "v_mfma_f32_16x16x32_bf16 v[8:11], v[140:143], v[220:223], v[8:11]\n\t"
    "s_setprio 0\n\t"
    "v_mfma_f32_16x16x32_bf16 v[12:15], v[140:143], v[224:227], v[12:15]\n\t"
    "s_setprio 1\n\t"
    "s_add_u32 %[sz], %[sz], 55296\n\t"
    "buffer_load_dwordx4 v[140:143], v228, %[srd], %[sz] offen sc1\n\t"
    "s_waitcnt vmcnt(36)\n\t"
    "v_mfma_f32_16x16x32_bf16 v[16:19], v[144:147], v[212:215], v[16:19]\n\t"
    "v_mfma_f32_16x16x32_bf16 v[20:23], v[144:147], v[216:219], v[20:23]\n\t"
    "s_setprio 0\n\t"
    "v_mfma_f32_16x16x32_bf16 v[24:27], v[144:147], v[220:223], v[24:27]\n\t"
    "s_setprio 1\n\t"
    "v_mfma_f32_16x16x32_bf16 v[28:31], v[144:147], v[224:227], v[28:31]\n\t"
    "buffer_load_dwordx4 v[144:147], v229, %[srd], %[sz] offen sc1\n\t"
    "s_waitcnt vmcnt(35)\n\t"
    "v_mfma_f32_16x16x32_bf16 v[32:35], v[148:151], v[212:215], v[32:35]\n\t"
    "s_setprio 0\n\t"
    "v_mfma_f32_16x16x32_bf16 v[36:39], v[148:151], v[216:219], v[36:39]\n\t"
    "s_setprio 1\n\t"
    "v_mfma_f32_16x16x32_bf16 v[40:43], v[148:151], v[220:223], v[40:43]\n\t"
    "v_mfma_f32_16x16x32_bf16 v[44:47], v[148:151], v[224:227], v[44:47]\n\t"
    "buffer_load_dwordx4 v[148:151], v230, %[srd], %[sz] offen sc1\n\t"
    "s_waitcnt vmcnt(26)\n\t"
    "ds_read_b128 v[196:199], v237 offset:0\n\t"
    "s_waitcnt vmcnt(34)\n\t"
    "s_setprio 0\n\t"
    "v_mfma_f32_16x16x32_bf16 v[48:51], v[152:155], v[212:215], v[48:51]\n\t"
    "s_setprio 1\n\t"
    "v_mfma_f32_16x16x32_bf16 v[52:55], v[152:155], v[216:219], v[52:55]\n\t"
    "v_mfma_f32_16x16x32_bf16 v[56:59], v[152:155], v[220:223], v[56:59]\n\t"
    "s_setprio 0\n\t"
    "v_mfma_f32_16x16x32_bf16 v[60:63], v[152:155], v[224:227], v[60:63]\n\t"
    "s_setprio 1\n\t"
    "buffer_load_dwordx4 v[152:155], v231, %[srd], %[sz] offen sc1\n\t"
    "ds_read_b128 v[200:203], v237 offset:2048\n\t"
    "s_waitcnt vmcnt(33)\n\t"
    "v_mfma_f32_16x16x32_bf16 v[64:67], v[156:159], v[212:215], v[64:67]\n\t"
    "v_mfma_f32_16x16x32_bf16 v[68:71], v[156:159], v[216:219], v[68:71]\n\t"
    "s_setprio 0\n\t"
    "v_mfma_f32_16x16x32_bf16 v[72:75], v[156:159], v[220:223], v[72:75]\n\t"
    "s_setprio 1\n\t"
    "v_mfma_f32_16x16x32_bf16 v[76:79], v[156:159], v[224:227], v[76:79]\n\t"
    "buffer_load_dwordx4 v[156:159], v232, %[srd], %[sz] offen sc1\n\t"
    "ds_read_b128 v[204:207], v237 offset:4096\n\t"
    "s_waitcnt vmcnt(32)\n\t"
    "v_mfma_f32_16x16x32_bf16 v[80:83], v[160:163], v[212:215], v[80:83]\n\t"
    "s_setprio 0\n\t"
    "v_mfma_f32_16x16x32_bf16 v[84:87], v[160:163], v[216:219], v[84:87]\n\t"
    "s_setprio 1\n\t"
    "v_mfma_f32_16x16x32_bf16 v[88:91], v[160:163], v[220:223], v[88:91]\n\t"
    "v_mfma_f32_16x16x32_bf16 v[92:95], v[160:163], v[224:227], v[92:95]\n\t"
    "buffer_load_dwordx4 v[160:163], v233, %[srd], %[sz] offen sc1\n\t"
    "ds_read_b128 v[208:211], v237 offset:6144\n\t"
    "s_waitcnt vmcnt(31)\n\t"
    "s_setprio 0\n\t"
    "v_mfma_f32_16x16x32_bf16 v[96:99], v[164:167], v[212:215], v[96:99]\n\t"
    "s_setprio 1\n\t"
    "v_mfma_f32_16x16x32_bf16 v[100:103], v[164:167], v[216:219], v[100:103]\n\t"
    "v_mfma_f32_16x16x32_bf16 v[104:107], v[164:167], v[220:223], v[104:107]\n\t"
    "s_setprio 0\n\t"
    "v_mfma_f32_16x16x32_bf16 v[108:111], v[164:167], v[224:227], v[108:111]\n\t"
    "s_setprio 1\n\t"
    "buffer_load_dwordx4 v[164:167], v234, %[srd], %[sz] offen sc1\n\t"
    "s_waitcnt lgkmcnt(0)\n\t"
    "s_waitcnt vmcnt(29)\n\t"
    "v_mfma_f32_16x16x32_bf16 v[0:3], v[168:171], v[196:199], v[0:3]\n\t"
    "v_mfma_f32_16x16x32_bf16 v[4:7], v[168:171], v[200:203], v[4:7]\n\t"
    "s_setprio 0\n\t"
    "v_mfma_f32_16x16x32_bf16 v[8:11], v[168:171], v[204:207], v[8:11]\n\t"
    "s_setprio 1\n\t"
    "v_mfma_f32_16x16x32_bf16 v[12:15], v[168:171], v[208:211], v[12:15]\n\t"
    "s_add_u32 %[sz], %[sz], 55296\n\t"
    "buffer_load_dwordx4 v[168:171], v228, %[srd], %[sz] offen sc1\n\t"
    "s_waitcnt vmcnt(29)\n\t"
    "v_mfma_f32_16x16x32_bf16 v[16:19], v[172:175], v[196:199], v[16:19]\n\t"
    "s_setprio 0\n\t"
    "v_mfma_f32_16x16x32_bf16 v[20:23], v[172:175], v[200:203], v[20:23]\n\t"
    "s_setprio 1\n\t"
    "v_mfma_f32_16x16x32_bf16 v[24:27], v[172:175], v[204:207], v[24:27]\n\t"
    "v_mfma_f32_16x16x32_bf16 v[28:31], v[172:175], v[208:211], v[28:31]\n\t"
    "buffer_load_dwordx4 v[172:175], v229, %[srd], %[sz] offen sc1\n\t"
    "s_waitcnt vmcnt(29)\n\t"
    "s_setprio 0\n\t"
    "v_mfma_f32_16x16x32_bf16 v[32:35], v[176:179], v[196:199], v[32:35]\n\t"
    "s_setprio 1\n\t"
    "v_mfma_f32_16x16x32_bf16 v[36:39], v[176:179], v[200:203], v[36:39]\n\t"
    "v_mfma_f32_16x16x32_bf16 v[40:43], v[176:179], v[204:207], v[40:43]\n\t"
    "s_setprio 0\n\t"
    "v_mfma_f32_16x16x32_bf16 v[44:47], v[176:179], v[208:211], v[44:47]\n\t"
    "s_setprio 1\n\t"
    "buffer_load_dwordx4 v[176:179], v230, %[srd], %[sz] offen sc1\n\t"
    "ds_read_b128 v[212:215], v238 offset:0\n\t"
    "s_waitcnt vmcnt(29)\n\t"
    "v_mfma_f32_16x16x32_bf16 v[48:51], v[180:183], v[196:199], v[48:51]\n\t"
    "v_mfma_f32_16x16x32_bf16 v[52:55], v[180:183], v[200:203], v[52:55]\n\t"
    "s_setprio 0\n\t"
    "v_mfma_f32_16x16x32_bf16 v[56:59], v[180:183], v[204:207], v[56:59]\n\t"
    "s_setprio 1\n\t"
    "v_mfma_f32_16x16x32_bf16 v[60:63], v[180:183], v[208:211], v[60:63]\n\t"
    "buffer_load_dwordx4 v[180:183], v231, %[srd], %[sz] offen sc1\n\t"
    "ds_read_b128 v[216:219], v238 offset:2048\n\t"
    "s_waitcnt vmcnt(29)\n\t"
    "v_mfma_f32_16x16x32_bf16 v[64:67], v[184:187], v[196:199], v[64:67]\n\t"
    "s_setprio 0\n\t"
    "v_mfma_f32_16x16x32_bf16 v[68:71], v[184:187], v[200:203], v[68:71]\n\t"
    "s_setprio 1\n\t"
    "v_mfma_f32_16x16x32_bf16 v[72:75], v[184:187], v[204:207], v[72:75]\n\t"
    "v_mfma_f32_16x16x32_bf16 v[76:79], v[184:187], v[208:211], v[76:79]\n\t"
    "buffer_load_dwordx4 v[184:187], v232, %[srd], %[sz] offen sc1\n\t"
    "ds_read_b128 v[220:223], v238 offset:4096\n\t"
    "s_waitcnt vmcnt(29)\n\t"
    "s_setprio 0\n\t"
    "v_mfma_f32_16x16x32_bf16 v[80:83], v[188:191], v[196:199], v[80:83]\n\t"
    "s_setprio 1\n\t"
    "v_mfma_f32_16x16x32_bf16 v[84:87], v[188:191], v[200:203], v[84:87]\n\t"
    "v_mfma_f32_16x16x32_bf16 v[88:91], v[188:191], v[204:207], v[88:91]\n\t"
    "s_setprio 0\n\t"
    "v_mfma_f32_16x16x32_bf16 v[92:95], v[188:191], v[208:211], v[92:95]\n\t"
    "s_setprio 1\n\t"
    "buffer_load_dwordx4 v[188:191], v233, %[srd], %[sz] offen sc1\n\t"
    "ds_read_b128 v[224:227], v238 offset:6144\n\t"
    "s_waitcnt vmcnt(29)\n\t"
    "v_mfma_f32_16x16x32_bf16 v[96:99], v[192:195], v[196:199], v[96:99]\n\t"
    "v_mfma_f32_16x16x32_bf16 v[100:103], v[192:195], v[200:203], v[100:103]\n\t"
    "s_setprio 0\n\t"
    "v_mfma_f32_16x16x32_bf16 v[104:107], v[192:195], v[204:207], v[104:107]\n\t"
    "s_setprio 1\n\t"
    "v_mfma_f32_16x16x32_bf16 v[108:111], v[192:195], v[208:211], v[108:111]\n\t"
    "buffer_load_dwordx4 v[192:195], v234, %[srd], %[sz] offen sc1\n\t"
    "s_waitcnt lgkmcnt(0)\n\t"
    "s_waitcnt vmcnt(28)\n\t"
    "v_mfma_f32_16x16x32_bf16 v[0:3], v[112:115], v[212:215], v[0:3]\n\t"
    "s_setprio 0\n\t"
    "v_mfma_f32_16x16x32_bf16 v[4:7], v[112:115], v[216:219], v[4:7]\n\t"
    "s_setprio 1\n\t"
    "v_mfma_f32_16x16x32_bf16 v[8:11], v[112:115], v[220:223], v[8:11]\n\t"
    "v_mfma_f32_16x16x32_bf16 v[12:15], v[112:115], v[224:227], v[12:15]\n\t"
    "s_waitcnt vmcnt(26)\n\t"
    "s_mov_b32 %[t], 262144\n\t"
    ".LLS_%=:\n\t"
    "v_cmp_ne_u64 vcc, v[240:241], v[242:243]\n\t"
    "s_nop 4\n\t"
    "s_cbranch_vccz .LLT_%=\n\t"
    "s_sleep 4\n\t"
    "buffer_load_dwordx2 v[240:241], v239, %[srd], 0 offen sc1\n\t"
    "s_waitcnt vmcnt(0)\n\t"
    "s_sub_u32 %[t], %[t], 1\n\t"
    "s_cmp_lg_u32 %[t], 0\n\t"
    "s_cbranch_scc1 .LLS_%=\n\t"
    ".LLT_%=:\n\t"
    "s_setprio 0\n\t"
    "v_mfma_f32_16x16x32_bf16 v[16:19], v[116:119], v[212:215], v[16:19]\n\t"
    "s_setprio 1\n\t"
    "v_mfma_f32_16x16x32_bf16 v[20:23], v[116:119], v[216:219], v[20:23]\n\t"
    "v_mfma_f32_16x16x32_bf16 v[24:27], v[116:119], v[220:223], v[24:27]\n\t"
    "s_setprio 0\n\t"
    "v_mfma_f32_16x16x32_bf16 v[28:31], v[116:119], v[224:227], v[28:31]\n\t"
    "s_setprio 1\n\t"
    "buffer_load_dwordx4 v[112:115], v244, %[srd], %[sz1] offen offset:0 sc1\n\t"
    "s_waitcnt vmcnt(25)\n\t"
    "v_mfma_f32_16x16x32_bf16 v[32:35], v[120:123], v[212:215], v[32:35]\n\t"
    "v_mfma_f32_16x16x32_bf16 v[36:39], v[120:123], v[216:219], v[36:39]\n\t"
    "s_setprio 0\n\t"
    "v_mfma_f32_16x16x32_bf16 v[40:43], v[120:123], v[220:223], v[40:43]\n\t"
    "s_setprio 1\n\t"
    "v_mfma_f32_16x16x32_bf16 v[44:47], v[120:123], v[224:227], v[44:47]\n\t"
    "buffer_load_dwordx4 v[116:119], v244, %[srd], %[sz1] offen offset:1024 sc1\n\t"
    "s_waitcnt vmcnt(16)\n\t"
    "ds_read_b128 v[196:199], v237 offset:8192\n\t"
    "s_waitcnt vmcnt(24)\n\t"
    "v_mfma_f32_16x16x32_bf16 v[48:51], v[124:127], v[212:215], v[48:51]\n\t"
    "s_setprio 0\n\t"
    "v_mfma_f32_16x16x32_bf16 v[52:55], v[124:127], v[216:219], v[52:55]\n\t"
    "s_setprio 1\n\t"
    "v_mfma_f32_16x16x32_bf16 v[56:59], v[124:127], v[220:223], v[56:59]\n\t"
    "v_mfma_f32_16x16x32_bf16 v[60:63], v[124:127], v[224:227], v[60:63]\n\t"
    "buffer_load_dwordx4 v[120:123], v244, %[srd], %[sz1] offen offset:2048 sc1\n\t"
    "ds_read_b128 v[200:203], v237 offset:10240\n\t"
    "s_waitcnt vmcnt(23)\n\t"
    "s_setprio 0\n\t"
    "v_mfma_f32_16x16x32_bf16 v[64:67], v[128:131], v[212:215], v[64:67]\n\t"
    "s_setprio 1\n\t"
    "v_mfma_f32_16x16x32_bf16 v[68:71], v[128:131], v[216:219], v[68:71]\n\t"
    "v_mfma_f32_16x16x32_bf16 v[72:75], v[128:131], v[220:223], v[72:75]\n\t"
    "s_setprio 0\n\t"
    "v_mfma_f32_16x16x32_bf16 v[76:79], v[128:131], v[224:227], v[76:79]\n\t"
    "s_setprio 1\n\t"
    "buffer_load_dwordx4 v[124:127], v244, %[srd], %[sz1] offen offset:3072 sc1\n\t"
    "ds_read_b128 v[204:207], v237 offset:12288\n\t"
    "s_waitcnt vmcnt(22)\n\t"
    "v_mfma_f32_16x16x32_bf16 v[80:83], v[132:135], v[212:215], v[80:83]\n\t"
    "v_mfma_f32_16x16x32_bf16 v[84:87], v[132:135], v[216:219], v[84:87]\n\t"
    "s_setprio 0\n\t"
    "v_mfma_f32_16x16x32_bf16 v[88:91], v[132:135], v[220:223], v[88:91]\n\t"
    "s_setprio 1\n\t"
    "v_mfma_f32_16x16x32_bf16 v[92:95], v[132:135], v[224:227], v[92:95]\n\t"
    "buffer_load_dword v128, v245, %[srd], %[g0] offen sc1\n\t"
    "buffer_load_dword v129, v245, %[srd], %[g1] offen sc1\n\t"
    "buffer_load_dword v130, v245, %[srd], %[g2] offen sc1\n\t"
    "buffer_load_dword v131, v245, %[srd], %[g3] offen sc1\n\t"
    "ds_read_b128 v[208:211], v237 offset:14336\n\t"
    "s_waitcnt vmcnt(24)\n\t"
    "v_mfma_f32_16x16x32_bf16 v[96:99], v[136:139], v[212:215], v[96:99]\n\t"
    "s_setprio 0\n\t"
    "v_mfma_f32_16x16x32_bf16 v[100:103], v[136:139], v[216:219], v[100:103]\n\t"
    "s_setprio 1\n\t"
    "v_mfma_f32_16x16x32_bf16 v[104:107], v[136:139], v[220:223], v[104:107]\n\t"
    "v_mfma_f32_16x16x32_bf16 v[108:111], v[136:139], v[224:227], v[108:111]\n\t"
    "s_waitcnt lgkmcnt(0)\n\t"
    "s_waitcnt vmcnt(21)\n\t"
    "s_setprio 0\n\t"
    "v_mfma_f32_16x16x32_bf16 v[0:3], v[140:143], v[196:199], v[0:3]\n\t"
    "s_setprio 1\n\t"
    "v_mfma_f32_16x16x32_bf16 v[4:7], v[140:143], v[200:203], v[4:7]\n\t"
    "v_mfma_f32_16x16x32_bf16 v[8:11], v[140:143], v[204:207], v[8:11]\n\t"
    "s_setprio 0\n\t"
    "v_mfma_f32_16x16x32_bf16 v[12:15], v[140:143], v[208:211], v[12:15]\n\t"
    "s_setprio 1\n\t"
    "buffer_load_dwordx4 v[140:143], v246, %[srd], %[swf] offen offset:0 sc1\n\t"
    "s_waitcnt vmcnt(21)\n\t"
    "v_mfma_f32_16x16x32_bf16 v[16:19], v[144:147], v[196:199], v[16:19]\n\t"
    "v_mfma_f32_16x16x32_bf16 v[20:23], v[144:147], v[200:203], v[20:23]\n\t"
    "s_setprio 0\n\t"
    "v_mfma_f32_16x16x32_bf16 v[24:27], v[144:147], v[204:207], v[24:27]\n\t"
    "s_setprio 1\n\t"
    "v_mfma_f32_16x16x32_bf16 v[28:31], v[144:147], v[208:211], v[28:31]\n\t"
    "buffer_load_dwordx4 v[144:147], v246, %[srd], %[swf] offen offset:1024 sc1\n\t"
    "s_waitcnt vmcnt(21)\n\t"
    "v_mfma_f32_16x16x32_bf16 v[32:35], v[148:151], v[196:199], v[32:35]\n\t"
    "s_setprio 0\n\t"
    "v_mfma_f32_16x16x32_bf16 v[36:39], v[148:151], v[200:203], v[36:39]\n\t"
    "s_setprio 1\n\t"
    "v_mfma_f32_16x16x32_bf16 v[40:43], v[148:151], v[204:207], v[40:43]\n\t"
    "v_mfma_f32_16x16x32_bf16 v[44:47], v[148:151], v[208:211], v[44:47]\n\t"
    "buffer_load_dwordx4 v[148:151], v246, %[srd], %[swf] offen offset:2048 sc1\n\t"
    "ds_read_b128 v[212:215], v238 offset:8192\n\t"
    "s_waitcnt vmcnt(21)\n\t"
    "s_setprio 0\n\t"
    "v_mfma_f32_16x16x32_bf16 v[48:51], v[152:155], v[196:199], v[48:51]\n\t"
    "s_setprio 1\n\t"
    "v_mfma_f32_16x16x32_bf16 v[52:55], v[152:155], v[200:203], v[52:55]\n\t"
    "v_mfma_f32_16x16x32_bf16 v[56:59], v[152:155], v[204:207], v[56:59]\n\t"
    "s_setprio 0\n\t"
    "v_mfma_f32_16x16x32_bf16 v[60:63], v[152:155], v[208:211], v[60:63]\n\t"
    "s_setprio 1\n\t"
    "buffer_load_dwordx4 v[152:155], v246, %[srd], %[swf] offen offset:3072 sc1\n\t"
    "ds_read_b128 v[216:219], v238 offset:10240\n\t"
    "s_waitcnt vmcnt(21)\n\t"
    "v_mfma_f32_16x16x32_bf16 v[64:67], v[156:159], v[196:199], v[64:67]\n\t"
    "v_mfma_f32_16x16x32_bf16 v[68:71], v[156:159], v[200:203], v[68:71]\n\t"
    "s_setprio 0\n\t"
    "v_mfma_f32_16x16x32_bf16 v[72:75], v[156:159], v[204:207], v[72:75]\n\t"
    "s_setprio 1\n\t"
    "v_mfma_f32_16x16x32_bf16 v[76:79], v[156:159], v[208:211], v[76:79]\n\t"
    "s_add_u32 %[t], %[swf], 4096\n\t"
    "buffer_load_dwordx4 v[156:159], v246, %[srd], %[t] offen sc1\n\t"
    "ds_read_b128 v[220:223], v238 offset:12288\n\t"
    "s_waitcnt vmcnt(21)\n\t"
    "v_mfma_f32_16x16x32_bf16 v[80:83], v[160:163], v[196:199], v[80:83]\n\t"
    "s_setprio 0\n\t"
    "v_mfma_f32_16x16x32_bf16 v[84:87], v[160:163], v[200:203], v[84:87]\n\t"
    "s_setprio 1\n\t"
    "v_mfma_f32_16x16x32_bf16 v[88:91], v[160:163], v[204:207], v[88:91]\n\t"
    "v_mfma_f32_16x16x32_bf16 v[92:95], v[160:163], v[208:211], v[92:95]\n\t"
    "s_add_u32 %[swf], %[swf], 40960\n\t"
    "buffer_load_dwordx4 v[160:163], v246, %[srd], %[swf] offen offset:0 sc1\n\t"
    "ds_read_b128 v[224:227], v238 offset:14336\n\t"
    "s_waitcnt vmcnt(21)\n\t"
    "s_setprio 0\n\t"
    "v_mfma_f32_16x16x32_bf16 v[96:99], v[164:167], v[196:199], v[96:99]\n\t"
    "s_setprio 1\n\t"
    "v_mfma_f32_16x16x32_bf16 v[100:103], v[164:167], v[200:203], v[100:103]\n\t"
    "v_mfma_f32_16x16x32_bf16 v[104:107], v[164:167], v[204:207], v[104:107]\n\t"
    "s_setprio 0\n\t"
    "v_mfma_f32_16x16x32_bf16 v[108:111], v[164:167], v[208:211], v[108:111]\n\t"
    "s_setprio 1\n\t"
    "buffer_load_dwordx4 v[164:167], v246, %[srd], %[swf] offen offset:1024 sc1\n\t"
    "s_waitcnt lgkmcnt(0)\n\t"
    "s_waitcnt vmcnt(21)\n\t"
    "v_mfma_f32_16x16x32_bf16 v[0:3], v[168:171], v[212:215], v[0:3]\n\t"
    "v_mfma_f32_16x16x32_bf16 v[4:7], v[168:171], v[216:219], v[4:7]\n\t"
    "s_setprio 0\n\t"
    "v_mfma_f32_16x16x32_bf16 v[8:11], v[168:171], v[220:223], v[8:11]\n\t"
    "s_setprio 1\n\t"
    "v_mfma_f32_16x16x32_bf16 v[12:15], v[168:171], v[224:227], v[12:15]\n\t"
    "buffer_load_dwordx4 v[196:199], v246, %[srd], %[swf] offen offset:2048 sc1\n\t"
    "s_waitcnt vmcnt(21)\n\t"
    "v_mfma_f32_16x16x32_bf16 v[16:19], v[172:175], v[212:215], v[16:19]\n\t"
    "s_setprio 0\n\t"
    "v_mfma_f32_16x16x32_bf16 v[20:23], v[172:175], v[216:219], v[20:23]\n\t"
    "s_setprio 1\n\t"
    "v_mfma_f32_16x16x32_bf16 v[24:27], v[172:175], v[220:223], v[24:27]\n\t"
    "v_mfma_f32_16x16x32_bf16 v[28:31], v[172:175], v[224:227], v[28:31]\n\t"
    "buffer_load_dwordx4 v[200:203], v246, %[srd], %[swf] offen offset:3072 sc1\n\t"
    "s_waitcnt vmcnt(21)\n\t"
    "s_setprio 0\n\t"
    "v_mfma_f32_16x16x32_bf16 v[32:35], v[176:179], v[212:215], v[32:35]\n\t"
    "s_setprio 1\n\t"
    "v_mfma_f32_16x16x32_bf16 v[36:39], v[176:179], v[216:219], v[36:39]\n\t"
    "v_mfma_f32_16x16x32_bf16 v[40:43], v[176:179], v[220:223], v[40:43]\n\t"
    "s_setprio 0\n\t"
    "v_mfma_f32_16x16x32_bf16 v[44:47], v[176:179], v[224:227], v[44:47]\n\t"
    "s_add_u32 %[t], %[swf], 4096\n\t"
    "buffer_load_dwordx4 v[204:207], v246, %[srd], %[t] offen sc1\n\t"
    "s_waitcnt vmcnt(21)\n\t"
    "s_setprio 1\n\t"
    "v_mfma_f32_16x16x32_bf16 v[48:51], v[180:183], v[212:215], v[48:51]\n\t"
    "v_mfma_f32_16x16x32_bf16 v[52:55], v[180:183], v[216:219], v[52:55]\n\t"
    "s_setprio 0\n\t"
    "v_mfma_f32_16x16x32_bf16 v[56:59], v[180:183], v[220:223], v[56:59]\n\t"
    "s_setprio 1\n\t"
    "v_mfma_f32_16x16x32_bf16 v[60:63], v[180:183], v[224:227], v[60:63]\n\t"
    "s_add_u32 %[swf], %[swf], 40960\n\t"
    "buffer_load_dwordx4 v[208:211], v246, %[srd], %[swf] offen offset:0 sc1\n\t"
    "s_waitcnt vmcnt(21)\n\t"
    "v_mfma_f32_16x16x32_bf16 v[64:67], v[184:187], v[212:215], v[64:67]\n\t"
    "s_setprio 0\n\t"
    "v_mfma_f32_16x16x32_bf16 v[68:71], v[184:187], v[216:219], v[68:71]\n\t"
    "s_setprio 1\n\t"
    "v_mfma_f32_16x16x32_bf16 v[72:75], v[184:187], v[220:223], v[72:75]\n\t"
    "v_mfma_f32_16x16x32_bf16 v[76:79], v[184:187], v[224:227], v[76:79]\n\t"
    "buffer_load_dwordx4 v[168:171], v246, %[srd], %[swf] offen offset:1024 sc1\n\t"
    "s_waitcnt vmcnt(21)\n\t"
    "s_setprio 0\n\t"
    "v_mfma_f32_16x16x32_bf16 v[80:83], v[188:191], v[212:215], v[80:83]\n\t"
    "s_setprio 1\n\t"
    "v_mfma_f32_16x16x32_bf16 v[84:87], v[188:191], v[216:219], v[84:87]\n\t"
    "v_mfma_f32_16x16x32_bf16 v[88:91], v[188:191], v[220:223], v[88:91]\n\t"
    "s_setprio 0\n\t"
    "v_mfma_f32_16x16x32_bf16 v[92:95], v[188:191], v[224:227], v[92:95]\n\t"
    "buffer_load_dwordx4 v[172:175], v246, %[srd], %[swf] offen offset:2048 sc1\n\t"
    "s_waitcnt vmcnt(21)\n\t"
    "s_setprio 1\n\t"
    "v_mfma_f32_16x16x32_bf16 v[96:99], v[192:195], v[212:215], v[96:99]\n\t"
    "v_mfma_f32_16x16x32_bf16 v[100:103], v[192:195], v[216:219], v[100:103]\n\t"
    "s_setprio 0\n\t"
    "v_mfma_f32_16x16x32_bf16 v[104:107], v[192:195], v[220:223], v[104:107]\n\t"
    "s_setprio 1\n\t"
    "v_mfma_f32_16x16x32_bf16 v[108:111], v[192:195], v[224:227], v[108:111]\n\t"
    "buffer_load_dwordx4 v[176:179], v246, %[srd], %[swf] offen offset:3072 sc1\n\t"
    "s_add_u32 %[t], %[swf], 4096\n\t"
    "buffer_load_dwordx4 v[180:183], v246, %[srd], %[t] offen sc1\n\t"
    "s_mov_b32 m0, %[m0s]\n\t"
    "s_waitcnt vmcnt(0)\n\t"
    "s_nop 7\n\t"
      : "+{v[0:15]}"(ac[0]), "+{v[16:31]}"(ac[1]), "={v[32:47]}"(ac[2]), "+{v[48:63]}"(ac[3]), "+{v[64:79]}"(ac[4]), "+{v[80:95]}"(ac[5]), "+{v[96:111]}"(ac[6]),
        "={v[112:127]}"(zq), "={v[128:131]}"(gm), "={v[140:155]}"(wq0), "={v[156:167]}"(wq1), "={v[196:211]}"(wq2), "={v[168:183]}"(wq3), [swf] "+s"(swf), [sz] "+s"(sz), [swd] "+s"(swd), [t] "=&s"(t), [m0s] "=&s"(m0s)
      : [srd] "s"(srd), [srw] "s"(srw), [ring] "s"(ring),
        "{v228}"(vz[0]), "{v229}"(vz[1]), "{v230}"(vz[2]), "{v231}"(vz[3]), "{v232}"(vz[4]), "{v233}"(vz[5]), "{v234}"(vz[6]),
        "{v235}"(vw0), "{v236}"(vw1), "{v237}"(vr0), "{v238}"(vr1), "{v239}"(vc), "{v244}"(vpa), "{v245}"(vgt), "{v246}"(vbs), "{v247}"(vfl), [sz1] "s"(WS_Z1), [g0] "s"(w * 64), [g1] "s"(((w + 1) & 3) * 64), [g2] "s"(((w + 2) & 3) * 64), [g3] "s"(((w + 3) & 3) * 64), [tlo] "s"((unsigned)tag), [thi] "s"((unsigned)(tag >> 32))
      : "memory", "scc", "vcc", "v132", "v133", "v134", "v135", "v136", "v137", "v138", "v139", "v184", "v185", "v186", "v187", "v188", "v189", "v190", "v191", "v192", "v193", "v194", "v195", "v212", "v213", "v214", "v215", "v216", "v217", "v218", "v219", "v220", "v221", "v222", "v223", "v224", "v225", "v226", "v227", "v240", "v241", "v242", "v243");
#pragma unroll
    for (int m = 0; m < 4; ++m) zf[m] = u32x4{zq[4 * m], zq[4 * m + 1], zq[4 * m + 2], zq[4 * m + 3]};
#pragma unroll
    for (int n = 0; n < 15; ++n) { const int k = n < 4 ? 4 * n : n < 7 ? 4 * (n - 4) : n < 11 ? 4 * (n - 7) : 4 * (n - 11);
      wf[n / 5][n % 5] = __builtin_bit_cast(bf16x8, n < 4 ? u32x4{wq0[k], wq0[k + 1], wq0[k + 2], wq0[k + 3]} : n < 7 ? u32x4{wq1[k], wq1[k + 1], wq1[k + 2], wq1[k + 3]}
                                                  : n < 11 ? u32x4{wq2[k], wq2[k + 1], wq2[k + 2], wq2[k + 3]} : u32x4{wq3[k], wq3[k + 1], wq3[k + 2], wq3[k + 3]}); } }
  asm volatile("" : "+v"(lane));
  __builtin_amdgcn_s_setprio(2);
  lds_arrive(cB, lane);
  lds_wait4(cB);
  { auto put = [&](unsigned a, const f32x16& s) {
#pragma unroll
      for (int m = 0; m < 4; ++m) *(LAS f32x4*)(lds + L_BX + a + m * 1024 + lane * 16) = f32x4{s[4 * m], s[4 * m + 1], s[4 * m + 2], s[4 * m + 3]}; };
    const int o1 = w == 3 ? 1 : w == 2 ? 0 : w + 1, o2 = w == 3 ? 2 : w == 0 ? 2 : w - 1;
    const unsigned a1 = o1 * 24576 + (w == 3 ? 16384 : 0), a2 = o2 * 24576 + (w == 3 ? 16384 : 8192);
    put(a1, ac[2]); put(a1 + 4096, ac[3]); put(a2, ac[4]); put(a2 + 4096, ac[5]);
    if (w < 3) put(3 * 24576 + w * 4096, ac[6]); else { put(16384, ac[0]); put(16384 + 4096, ac[1]); } }
  lds_arrive(cX, lane); lds_wait4(cX);
  if (w < 3) {
#pragma unroll
    for (int m = 0; m < 4; ++m) { f32x4 a = {ac[0][4 * m], ac[0][4 * m + 1], ac[0][4 * m + 2], ac[0][4 * m + 3]}, c = {ac[1][4 * m], ac[1][4 * m + 1], ac[1][4 * m + 2], ac[1][4 * m + 3]};
#pragma unroll
      for (int i = 0; i < 3; ++i) { a += *(const LAS f32x4*)(lds + L_BX + w * 24576 + i * 8192 + m * 1024 + lane * 16); c += *(const LAS f32x4*)(lds + L_BX + w * 24576 + i * 8192 + (4 + m) * 1024 + lane * 16); }
      const f32x4 za = {bflo(zf[m][0]), bfhi(zf[m][0]), bflo(zf[m][1]), bfhi(zf[m][1])}, zc = {bflo(zf[m][2]), bfhi(zf[m][2]), bflo(zf[m][3]), bfhi(zf[m][3])};
      st16(pk8(a * za, c * zc), rws, WS_U + pa + m * 1024); }
  } else {
#pragma unroll
    for (int m = 0; m < 4; ++m) { f32x4 a = {ac[6][4 * m], ac[6][4 * m + 1], ac[6][4 * m + 2], ac[6][4 * m + 3]};
#pragma unroll
      for (int i = 0; i < 3; ++i) a += *(const LAS f32x4*)(lds + L_BX + 3 * 24576 + i * 4096 + m * 1024 + lane * 16);
      const f32x4 za = {bflo(zf[m][0]), bfhi(zf[m][0]), bflo(zf[m][1]), bfhi(zf[m][1])};
      st8(pk4(a * za), rws, nu == 7 ? WS_U + pa + m * 1024 : OOB); }
  }
  SBAR();
  const unsigned vl = lane * 16, wfb = WS_WF + g * 5120, vl6 = w == 3 ? OOB : vl;
  auto ldw = [&](int i) {
#pragma unroll
    for (int j = 0; j < 5; ++j) wf[i][j] = ldfrag(rws, (i == 6 ? vl6 : vl) + j * 1024, wfb + (7 * w + i) * 40960); };
#pragma unroll
  for (int i = 3; i < 4; ++i) ldw(i);
  SBAR();
  publish_n<5 * (4 - 3)>(ws, WS_FU + (rb * 8 + g) * 128, tag, lane, cU, 4);
  f32x4 fa[5][4];
  {
    auto ldu = [&](int i) {
#pragma unroll
      for (int m = 0; m < 4; ++m) uf[i][m] = i == 3 ? ldfrag2(rws, w == 3 ? vl : lane * 8, w == 3 ? 8 : 512, WS_U + (rb * 27 + 7 * w + i) * 4096 + ((m + w) & 3) * 1024)
                                                    : ldfrag(rws, i == 6 ? vl6 : vl, WS_U + (rb * 27 + 7 * w + i) * 4096 + ((m + w) & 3) * 1024); };
    lds_wait4(cU);
#pragma unroll
    for (int n = 0; n < 20; ++n) dma16c(lds0 + w * 21504 + n * 1024, vl, srd, wfb + (27 + 4 * (n / 5) + w) * 40960 + (n % 5) * 1024);
    poll64(srd, WS_FU + (rb * 8 + (lane & 7)) * 128, tag);
    ldu(0); ldu(1); ldu(2); ldu(3); SBAR();
#pragma unroll
    for (int i = 0; i < 7; ++i) {
#pragma unroll
      for (int j = 0; j < 5; ++j)
#pragma unroll
        for (int m = 0; m < 4; ++m) fa[j][m] = mfma16(wf[i][j], uf[i][m], i ? fa[j][m] : f32x4{});
      SBAR();
      if (i + 4 < 7) ldw(i + 4);
      if (i + 4 < 7) ldu(i + 4);
      SBAR();
    }
  }
  {
    bf16x8 wf[5][5], of[5][4];
    auto rdw = [&](int i) { const int x = i;
#pragma unroll
      for (int j = 0; j < 5; ++j) wf[i][j] = *(const LAS bf16x8*)(lds + w * 21504 + x * 5120 + j * 1024 + vl); };
#pragma unroll
    for (int j = 0; j < 5; ++j) wf[4][j] = ldfrag(rws, vl + j * 1024, wfb + (27 + 16 + w) * 40960);
    rdw(0); rdw(1);
    SBAR();
#pragma unroll
    for (int m = 0; m < 4; ++m)
#pragma unroll
      for (int j = 0; j < 5; ++j) { fa[j][m] *= 1.0f - gm[m]; asm volatile("" : "+v"(fa[j][m])); }
    poll64(srd, WS_FO + (rb * 32 + ((lane & 8) ? 8 * w + (lane & 7) : 8 * ((lane & 7) >> 1) + 2 * w + (lane & 1))) * 128, tag);
    auto ldo = [&](int i) { const int x = i;
#pragma unroll
      for (int m = 0; m < 4; ++m) of[i][m] = i < 4 ? ldfrag(rws, vl, WS_O + (rb * 20 + 4 * x + w) * 4096 + ((m + w) & 3) * 1024) : ldfrag2(rws, lane * 8, 512, WS_O + (rb * 20 + 16 + w) * 4096 + ((m + w) & 3) * 1024); };
    auto mm = [&](int i) {
#pragma unroll
      for (int j = 0; j < 5; ++j)
#pragma unroll
        for (int m = 0; m < 4; ++m) fa[j][m] = mfma16(wf[i][j], of[i][m], fa[j][m]); };
    ldo(0); ldo(1); ldo(2); ldo(3); ldo(4); SBAR();
    mm(0); SBAR(); rdw(2); SBAR(); mm(1); SBAR(); rdw(3); SBAR();
    mm(2); mm(3); SBAR();
#pragma unroll
    for (int m = 3; m >= 0; --m) {
#pragma unroll
      for (int j = 0; j < 5; ++j) fa[j][m] = mfma16(wf[4][j], of[4][m], fa[j][m]);
      if (m < 3) {
#pragma unroll
        for (int j = 0; j < 5; ++j) *(LAS f32x4*)(lds + w * 21504 + m * 5120 + j * 1024 + vl) = fa[j][m + 1]; }
      SBAR(); }
  }
  lds_arrive(cF, lane); lds_wait4(cF);
  f32x4 fin[5];
#pragma unroll
  for (int j = 0; j < 5; ++j) { fin[j] = fa[j][0];
#pragma unroll
    for (int m = 1; m < 4; ++m) fin[j] += *(const LAS f32x4*)(lds + ((w - m) & 3) * 21504 + (m - 1) * 5120 + j * 1024 + vl); }
  const rsrc_t ro = mk_rsrc(out, 2048 * 1280); const unsigned oa = (64 * rb + 16 * w + i16) * 1280 + 160 * g + 40 * fq;
  __builtin_amdgcn_raw_buffer_store_b128(pk8(fin[0], fin[1]), ro, oa, 0, 0);
  __builtin_amdgcn_raw_buffer_store_b128(pk8(fin[2], fin[3]), ro, oa + 16, 0, 0);
  __builtin_amdgcn_raw_buffer_store_b64(pk4(fin[4]), ro, oa + 32, 0, 0);
}
extern "C" void kernel_launch(void* const* d_in, const int* in_sizes, int n_in, void* d_out, int out_size, void* d_ws, size_t ws_size, hipStream_t stream) {
  const __bf16* x = (const __bf16*)d_in[0]; const __bf16* wqkv = (const __bf16*)d_in[1]; const __bf16* wo = (const __bf16*)d_in[2];
  const __bf16* win = (const __bf16*)d_in[4]; const __bf16* wsp = (const __bf16*)d_in[5]; const __bf16* bsp = (const __bf16*)d_in[6];
  const __bf16* wout = (const __bf16*)d_in[7]; const __bf16* gatew = (const __bf16*)d_in[8];
  (void)hipFuncSetAttribute((const void*)gated_mix, hipFuncAttributeMaxDynamicSharedMemorySize, LDS_BYTES);
  hipLaunchKernelGGL(gated_mix, dim3(256), dim3(512), LDS_BYTES, stream, x, wqkv, win, wo, wout, gatew, wsp, bsp, (char*)d_ws, (__bf16*)d_out);
}
```
